```python
import torch

def _softmax(x, axis=-1):
    return torch.softmax(x, dim=axis)

def _gelu(x):
    return torch.nn.functional.gelu(x, approximate='tanh')

def _silu(x):
    return torch.nn.functional.silu(x)

def _sigmoid(x):
    return torch.sigmoid(x)

def _layernorm(x, axis=-1, eps=1e-05):
    m = x.mean(dim=axis, keepdim=True)
    v = x.var(dim=axis, keepdim=True, unbiased=False)
    return (x - m) / torch.sqrt(v + eps)

def _scatter_add(tgt, idx, src, axis=0):
    return tgt.clone().index_add_(axis, idx, src)

def _segment_sum(src, seg, n):
    out = torch.zeros((n,) + src.shape[1:], dtype=src.dtype)
    return out.index_add_(0, seg, src)

def _pad(x, pw):
    flat = tuple((v for pair in reversed(pw) for v in pair))
    return torch.nn.functional.pad(x, flat)
kernel_name = 'interleave_layers__memory_aug_recurrent_mem_tr__glu_gated_reglu__prompt'

def setup_inputs():

    _w = lambda a: (lambda t: t.to(torch.bfloat16) if t.is_floating_point() else t)(torch.as_tensor(a))

    def _A_setup():
        B, S, Dm = (1, 2048, 512)
        H, Dh = (32, 128)
        M, seg = (8, 32)
        seg = min(seg, S)
        S = S // seg * seg
        return {'x': _w((lambda *s: torch.randn(*s, dtype=torch.bfloat16) * 0.05)(B, S, Dm)), 'mem0': _w((lambda *s: torch.randn(*s, dtype=torch.bfloat16) * 0.2)(B, M, Dm)), 'Wq': _w((lambda *s: torch.randn(*s, dtype=torch.bfloat16) * 1.6)(Dm, H, Dh) * Dm ** (-0.5)), 'Wk': _w((lambda *s: torch.randn(*s, dtype=torch.bfloat16) * 1.6)(Dm, H, Dh) * Dm ** (-0.5)), 'Wv': _w((lambda *s: torch.randn(*s, dtype=torch.bfloat16) * 0.2)(Dm, H, Dh) * Dm ** (-0.5)), 'Wo': _w((lambda *s: torch.randn(*s, dtype=torch.bfloat16) * 0.2)(H, Dh, Dm) * (H * Dh) ** (-0.5)), 'seg_len': seg}
    _A_in = _A_setup()

    def _B_setup():
        B, S, D, F = (1, 2048, 512, 1792)
        return {'x': _w((lambda *s: torch.randn(*s, dtype=torch.bfloat16) * 0.05)(B, S, D)), 'W_gate': _w((lambda *s: torch.randn(*s, dtype=torch.bfloat16) * 0.2)(D, F) * (2.0 / D) ** 0.5), 'W_up': _w((lambda *s: torch.randn(*s, dtype=torch.bfloat16) * 0.2)(D, F) * (2.0 / D) ** 0.5), 'W_down': _w((lambda *s: torch.randn(*s, dtype=torch.bfloat16) * 0.2)(F, D) * (1.0 / F) ** 0.5)}
    _B_in = _B_setup()
    return {'A_x': _A_in['x'], 'A_mem0': _A_in['mem0'], 'A_Wq': _A_in['Wq'], 'A_Wk': _A_in['Wk'], 'A_Wv': _A_in['Wv'], 'A_Wo': _A_in['Wo'], 'A_seg_len': _A_in['seg_len'], 'B_x': _B_in['x'], 'B_W_gate': _B_in['W_gate'], 'B_W_up': _B_in['W_up'], 'B_W_down': _B_in['W_down']}

def reference(A_x, A_mem0, A_Wq, A_Wk, A_Wv, A_Wo, A_seg_len, B_x, B_W_gate, B_W_up, B_W_down):

    def _A_ref(x, mem0, Wq, Wk, Wv, Wo, seg_len):
        B, S, Dm = x.shape
        H, Dh = (Wq.shape[1], Wq.shape[2])
        M = mem0.shape[1]
        scale = Dh ** (-0.5)
        n_seg = S // seg_len
        L = M + seg_len
        idx = torch.arange(L)
        mask = (idx[:, None] < M) | (idx[:, None] >= idx[None, :])
        mem = mem0
        outs = []
        for s in range(n_seg):
            xs = x[:, s * seg_len:(s + 1) * seg_len, :].clone()
            h = torch.concatenate([mem, xs], axis=1)
            q = torch.einsum('btd,dhe->bhte', h, Wq)
            k = torch.einsum('btd,dhe->bhte', h, Wk)
            v = torch.einsum('btd,dhe->bhte', h, Wv)
            scores = torch.einsum('bhqe,bhke->bhqk', q, k) * scale
            scores = torch.where(mask[None, None], scores, -1000000000.0)
            p = _softmax(scores, axis=-1)
            o = torch.einsum('bhqk,bhke->bhqe', p, v)
            o = torch.einsum('bhte,hed->btd', o, Wo)
            mem = o[:, :M, :].clone()
            outs.append(o[:, M:, :])
        return torch.concatenate(outs, axis=1)

    def _B_ref(x, W_gate, W_up, W_down):
        gate = torch.einsum('bsd,df->bsf', x, W_gate)
        up = torch.einsum('bsd,df->bsf', x, W_up)
        gate = gate * (lambda x, dt: x.to(dt))(gate > 0, gate.dtype)
        h = gate * up
        return torch.einsum('bsf,fd->bsd', h, W_down)
    x = A_x
    x = x + _A_ref(_layernorm(x, axis=-1), A_mem0, A_Wq, A_Wk, A_Wv, A_Wo, A_seg_len)
    x = x + _B_ref(_layernorm(x, axis=-1), B_W_gate, B_W_up, B_W_down)
    x = x + _A_ref(_layernorm(x, axis=-1), A_mem0, A_Wq, A_Wk, A_Wv, A_Wo, A_seg_len)
    x = x + _B_ref(_layernorm(x, axis=-1), B_W_gate, B_W_up, B_W_down)
    return x
```

```cpp
#include <hip/hip_runtime.h>
#include <stdint.h>
#include <utility>

#define LAS __attribute__((address_space(3)))
#define GAS __attribute__((address_space(1)))
#define DI __device__ __forceinline__
#define RLX_AGENT __ATOMIC_RELAXED, __HIP_MEMORY_SCOPE_AGENT
typedef unsigned v4u __attribute__((ext_vector_type(4)));
typedef unsigned v2u __attribute__((ext_vector_type(2)));
typedef float f32x4 __attribute__((ext_vector_type(4)));
typedef float f32x2 __attribute__((ext_vector_type(2)));
typedef __bf16 bf16x2 __attribute__((ext_vector_type(2)));
typedef GAS unsigned gu32;
typedef GAS unsigned long long gu64;
typedef __amdgpu_buffer_rsrc_t rsrc_t;

constexpr int NSEG = 64, NUNIT = 128, FF = 1792;
constexpr int LDS_BYTES = 163840;
constexpr int OFF_TICKET = 0;
constexpr int OFF_ABORT = 2048;
constexpr int OFF_BOUND = 4096;
constexpr int OFF_HU = 8192;
constexpr int OFF_CNTO = 32768;
constexpr int OFF_CNTX1 = OFF_CNTO + 2 * 64 * 4 * 128;
constexpr int OFF_CNTH = OFF_CNTX1 + 2 * 128 * 128;
constexpr int OFF_CNTX2 = OFF_CNTH + 2 * 128 * 128;
constexpr int OFF_ACC = 262144;
constexpr int CTL_BYTES = OFF_ACC + 64 * 32768;
constexpr int OFF_X1 = 4 << 20;
constexpr int OFF_X2 = 8 << 20;
constexpr int OFF_O = 16 << 20;
constexpr int OFF_HB = 48 << 20;
constexpr int OFF_PROF = 96 << 20;
constexpr int WS_USED = 112 << 20;
static_assert(OFF_CNTX2 + 64 * 128 <= OFF_ACC, "control block");
#define HUPOS 5
#define BACKOFF 4
constexpr unsigned SPIN_CAP = 1u << 18;

struct Params {
  const void *x, *mem0, *wq, *wk, *wv, *wo, *wg, *wu, *wd;
  void* out;
  char* ws;
};

#define STAMPC(slot) do {} while (0)
#define STAMP(slot) do {} while (0)

DI unsigned rfl(unsigned x) { return __builtin_amdgcn_readfirstlane(x); }
DI unsigned cvtpk(float lo, float hi) { f32x2 v = {lo, hi}; bf16x2 b = __builtin_convertvector(v, bf16x2); return __builtin_bit_cast(unsigned, b); }
DI float bflo(unsigned w) { return __uint_as_float(w << 16); }
DI float bfhi(unsigned w) { return __uint_as_float(w & 0xffff0000u); }
DI float dot2(unsigned a, unsigned b, float c) { return __builtin_amdgcn_fdot2_f32_bf16(__builtin_bit_cast(bf16x2, a), __builtin_bit_cast(bf16x2, b), c, false); }
template <int CTRL> DI float dpp(float v) { return __builtin_bit_cast(float, __builtin_amdgcn_update_dpp(0, __builtin_bit_cast(int, v), CTRL, 0xf, 0xf, false)); }
DI float rows_max(float v) {
  auto a = __builtin_amdgcn_permlane16_swap(__float_as_uint(v), __float_as_uint(v), false, false);
  v = fmaxf(__uint_as_float(a[0]), __uint_as_float(a[1]));
  auto b = __builtin_amdgcn_permlane32_swap(__float_as_uint(v), __float_as_uint(v), false, false);
  return fmaxf(__uint_as_float(b[0]), __uint_as_float(b[1]));
}
DI float rows_sum(float v) {
  auto a = __builtin_amdgcn_permlane16_swap(__float_as_uint(v), __float_as_uint(v), false, false);
  v = __uint_as_float(a[0]) + __uint_as_float(a[1]);
  auto b = __builtin_amdgcn_permlane32_swap(__float_as_uint(v), __float_as_uint(v), false, false);
  return __uint_as_float(b[0]) + __uint_as_float(b[1]);
}
DI float row16_sum(float v) { v += dpp<0x128>(v); v += dpp<0x124>(v); v += dpp<0x122>(v); v += dpp<0x121>(v); return v; }
DI float row16_max(float v) { v = fmaxf(v, dpp<0x128>(v)); v = fmaxf(v, dpp<0x124>(v)); v = fmaxf(v, dpp<0x122>(v)); v = fmaxf(v, dpp<0x121>(v)); return v; }
DI float oct_sum(float v) { v += dpp<0xB1>(v); v += dpp<0x4E>(v); v += dpp<0x141>(v); return v; }

DI LAS char* lptr(unsigned a) { return (LAS char*)(uintptr_t)a; }
DI v4u lds128(unsigned a, int o = 0) { return *(const LAS v4u*)(lptr(a) + o); }
DI f32x4 lds128f(unsigned a, int o = 0) { return *(const LAS f32x4*)(lptr(a) + o); }
DI f32x2 lds64f(unsigned a, int o = 0) { return *(const LAS f32x2*)(lptr(a) + o); }
DI void sts128(unsigned a, int o, v4u v) { *(LAS v4u*)(lptr(a) + o) = v; }
DI void sts128f(unsigned a, int o, f32x4 v) { *(LAS f32x4*)(lptr(a) + o) = v; }
DI void sts64f(unsigned a, int o, f32x2 v) { *(LAS f32x2*)(lptr(a) + o) = v; }
DI void sts32(unsigned a, int o, unsigned v) { *(LAS unsigned*)(lptr(a) + o) = v; }
DI int lane_id() { int l; asm volatile("v_mbcnt_lo_u32_b32 %0, -1, 0\n\tv_mbcnt_hi_u32_b32 %0, -1, %0" : "=v"(l)); __builtin_assume(l >= 0 && l < 64); return l; }
DI rsrc_t mkrsrc(const void* ptr, unsigned bytes) { return __builtin_amdgcn_make_buffer_rsrc((void*)ptr, 0, bytes, 0x00020000); }
DI v4u ld_sc1(rsrc_t r, int voff, int soff) { return __builtin_amdgcn_raw_buffer_load_b128(r, voff, soff, 16); }
DI void st_sc1(v4u v, rsrc_t r, int voff, int soff) { __builtin_amdgcn_raw_buffer_store_b128(v, r, voff, soff, 16); }
#define BAR() asm volatile("s_waitcnt lgkmcnt(0)\n\ts_barrier" ::: "memory")
#define DRAIN() asm volatile("s_waitcnt vmcnt(0)" ::: "memory")
#define FENCE() asm volatile("" ::: "memory")

template <int... Is, class F> DI void sfor_(std::integer_sequence<int, Is...>, F&& f) { (f(std::integral_constant<int, Is>{}), ...); }
template <int N, class F> DI void sfor(F&& f) { sfor_(std::make_integer_sequence<int, N>{}, f); }

#define MF32(acc, A, B)  asm volatile("v_mfma_f32_16x16x32_bf16 %0, %1, %2, %0" : "+v"(acc) : "v"(A), "v"(B))
#define MF32Z(acc, A, B) asm volatile("v_mfma_f32_16x16x32_bf16 %0, %1, %2, 0" : "=&v"(acc) : "v"(A), "v"(B))
#define MF16(acc, A, B)  asm volatile("v_mfma_f32_16x16x16_bf16 %0, %1, %2, %0" : "+v"(acc) : "v"(A), "v"(B))
constexpr int slot_reg(int slot) { return slot < 64 ? 4 * slot : 128 + 4 * (slot - 64); }
template <int SLOT, bool Z> DI void mfWA(f32x4& acc, const v4u& x) {
  constexpr int R = slot_reg(SLOT);
  if constexpr (SLOT < 64) {
    if constexpr (Z) asm volatile("v_mfma_f32_16x16x32_bf16 %0, a[%c2:%c3], %1, 0" : "=&v"(acc) : "v"(x), "i"(R), "i"(R + 3));
    else asm volatile("v_mfma_f32_16x16x32_bf16 %0, a[%c2:%c3], %1, %0" : "+v"(acc) : "v"(x), "i"(R), "i"(R + 3));
  } else {
    if constexpr (Z) asm volatile("v_mfma_f32_16x16x32_bf16 %0, v[%c2:%c3], %1, 0" : "=&v"(acc) : "v"(x), "i"(R), "i"(R + 3));
    else asm volatile("v_mfma_f32_16x16x32_bf16 %0, v[%c2:%c3], %1, %0" : "+v"(acc) : "v"(x), "i"(R), "i"(R + 3));
  }
}
template <int SLOT, bool Z> DI void mfWB(f32x4& acc, const v4u& x) {
  static_assert(SLOT >= 64, "");
  constexpr int R = slot_reg(SLOT);
  if constexpr (Z) asm volatile("v_mfma_f32_16x16x32_bf16 %0, %1, v[%c2:%c3], 0" : "=&v"(acc) : "v"(x), "i"(R), "i"(R + 3));
  else asm volatile("v_mfma_f32_16x16x32_bf16 %0, %1, v[%c2:%c3], %0" : "+v"(acc) : "v"(x), "i"(R), "i"(R + 3));
}
template <int SLOT, bool Z> DI void mfWW(f32x4& acc) {
  constexpr int R = slot_reg(SLOT);
  if constexpr (Z) asm volatile("v_mfma_f32_16x16x32_bf16 %0, v[%c1:%c2], v[%c1:%c2], 0" : "=&v"(acc) : "i"(R), "i"(R + 3));
  else asm volatile("v_mfma_f32_16x16x32_bf16 %0, v[%c1:%c2], v[%c1:%c2], %0" : "+v"(acc) : "i"(R), "i"(R + 3));
}
template <int SLOT, int OFF, int HS> DI void tr_slot(unsigned a) {
  constexpr int R = slot_reg(SLOT);
  if constexpr (SLOT < 64)
    asm volatile("ds_read_b64_tr_b16 a[%c1:%c2], %0 offset:%c5\n\tds_read_b64_tr_b16 a[%c3:%c4], %0 offset:%c6"
                 :: "v"(a), "i"(R), "i"(R + 1), "i"(R + 2), "i"(R + 3), "i"(OFF), "i"(OFF + HS) : "memory");
  else
    asm volatile("ds_read_b64_tr_b16 v[%c1:%c2], %0 offset:%c5\n\tds_read_b64_tr_b16 v[%c3:%c4], %0 offset:%c6"
                 :: "v"(a), "i"(R), "i"(R + 1), "i"(R + 2), "i"(R + 3), "i"(OFF), "i"(OFF + HS) : "memory");
}
#define WAIT_LDS() asm volatile("s_waitcnt lgkmcnt(0)" ::: "memory")

DI bool wait_ge(gu32* w, unsigned want, gu32* abortw, unsigned* got = nullptr, unsigned mask = 0xffffffffu) {
  for (unsigned spins = 0;;) {
    const unsigned v = rfl(__hip_atomic_load(w, RLX_AGENT));
    if ((v & mask) >= want) { if (got) *got = v; FENCE(); return true; }
    if (++spins > SPIN_CAP) { __hip_atomic_store(abortw, 1u, RLX_AGENT); return false; }
    if ((spins & 127u) == 0u && rfl(__hip_atomic_load(abortw, RLX_AGENT)) != 0u) return false;
    __builtin_amdgcn_s_sleep(1);
  }
}
DI bool spin_fail(unsigned& spins, gu32* abortw) {
  if (++spins > SPIN_CAP) { __hip_atomic_store(abortw, 1u, RLX_AGENT); return true; }
  return (spins & 127u) == 0u && rfl(__hip_atomic_load(abortw, RLX_AGENT)) != 0u;
}
DI void signal_last(unsigned ldsword, gu32* cnt, int lane) {
  if (lane == 0) {
    const unsigned old = __hip_atomic_fetch_add((LAS unsigned*)lptr(ldsword), 1u, __ATOMIC_RELAXED, __HIP_MEMORY_SCOPE_WORKGROUP);
    if ((old & 3u) == 3u) __hip_atomic_fetch_add(cnt, 1u, RLX_AGENT);
  }
}

DI void glds16(const void* sbase, unsigned voff, unsigned dst) {
  unsigned keep;
  asm volatile("s_mov_b32 %0, m0\n\ts_mov_b32 m0, %3\n\ts_nop 0\n\tglobal_load_lds_dwordx4 %1, %2\n\ts_mov_b32 m0, %0"
               : "=&s"(keep) : "v"(voff), "s"(sbase), "s"(dst) : "memory");
}
template <int N> DI void wait_vm() { asm volatile("s_waitcnt vmcnt(%0)" :: "i"(N) : "memory"); }
template <int OFF, int HS> DI void tr_pair(unsigned a, v4u& f0, v4u& f1) {
  v2u r0, r1, r2, r3;
  asm volatile("ds_read_b64_tr_b16 %0, %4 offset:%5\n\tds_read_b64_tr_b16 %1, %4 offset:%6\n\t"
               "ds_read_b64_tr_b16 %2, %4 offset:%7\n\tds_read_b64_tr_b16 %3, %4 offset:%8\n\ts_waitcnt lgkmcnt(0)"
               : "=&v"(r0), "=&v"(r1), "=&v"(r2), "=&v"(r3) : "v"(a), "i"(OFF), "i"(OFF + HS), "i"(OFF + 8), "i"(OFF + HS + 8) : "memory");
  f0 = v4u{r0.x, r0.y, r1.x, r1.y};
  f1 = v4u{r2.x, r2.y, r3.x, r3.y};
}
template <int S0, int K0> DI void tr_quad(unsigned a) {
  sfor<8>([&](auto I) { constexpr int i = I >> 1, j = I & 1; tr_slot<S0 + 16 * j + K0 + i, 8192 * i + 8 * j, 1024>(a); });
  WAIT_LDS();
}
DI void chunk_issue(const char* src, unsigned ldb, unsigned slot, int wave, int lane) {
  const unsigned voff = (unsigned)(lane >> 4) * ldb + (unsigned)(((lane & 15) ^ ((lane >> 4) << 2)) << 4);
#pragma unroll
  for (int i = 0; i < 8; ++i) glds16(src + (size_t)(32 * wave + 4 * i) * ldb, voff, slot + (unsigned)(32 * wave + 4 * i) * 256u);
}

constexpr float SM_C = 0.08838834764831845f * 1.4426950408889634f;
constexpr unsigned ONES = 0x3f803f80u;

DI void role_H(const Params& p, const int role, const int wave, const int layer, const int rep, const int h) {
  const int lane = lane_id();
  const int t = lane & 15, g = lane >> 4;
  gu32* abortw = (gu32*)(p.ws + OFF_ABORT);
  const rsrc_t rws = mkrsrc(p.ws, WS_USED);
  int pidx = 127; (void)pidx;
  STAMP(0);
  constexpr int SQ = 0, SK = 32, SV = 64;
  v4u wo_last;
  constexpr unsigned XT = 32768;
  const unsigned WO = (unsigned)((wave + 2) % 5) * 32768u;
  const unsigned MISC = WO + 31744;
  constexpr unsigned MISC0 = 2 * 32768 + 31744, MISC1 = 3 * 32768 + 31744;
  constexpr unsigned STATS = MISC0 + 512, MAXW = MISC1 + 512, SIGW = MISC1 + 576, BNDW = MISC1 + 640;
  float csv0, csv1, bstat;
  {
    auto src_of = [&](int c) -> const char* {
      if (c < 4) return (const char*)p.wk + ((size_t)(128 * c) * 4096 + h * 128) * 2;
      if (c < 8) return (const char*)p.wv + ((size_t)(128 * (c - 4)) * 4096 + h * 128) * 2;
      if (c < 12) return (const char*)p.wq + ((size_t)(128 * (c - 8)) * 4096 + h * 128) * 2;
      return (const char*)p.wo + ((size_t)(h * 128) * 512 + (c - 12) * 128) * 2;
    };
    const int q = (lane >> 2) & 3, pp = lane & 3;
    const unsigned trb = (unsigned)(8 * g + q) * 256u + (unsigned)(((4 * wave + pp) ^ (q << 2)) << 4);
#pragma unroll
    for (int c = 0; c < 4; ++c) chunk_issue(src_of(c), 8192, (unsigned)(c % 5) * 32768u, wave, lane);
    sfor<12>([&](auto C) {
      constexpr int c = C;
      wait_vm<24>();
      BAR();
      chunk_issue(src_of(c + 4), c + 4 < 12 ? 8192 : 1024, (unsigned)((c + 4) % 5) * 32768u, wave, lane);
      tr_quad<(c < 4 ? SK : c < 8 ? SV : SQ), 4 * (c & 3)>(trb + (unsigned)(c % 5) * 32768u);
    });
    v4u ones = {ONES, ONES, ONES, ONES};
    f32x4 cs[6], gram;
    asm volatile("s_nop 1" : "+v"(ones));
    sfor<32>([&](auto I) {
      constexpr int kk = I >> 1, j = I & 1;
      mfWA<SQ + 16 * j + kk, kk == 0>(cs[j], ones); mfWA<SK + 16 * j + kk, kk == 0>(cs[2 + j], ones); mfWB<SV + 16 * j + kk, kk == 0>(cs[4 + j], ones);
      mfWW<SV + 16 * j + kk, I == 0>(gram);
    });
    asm volatile("s_nop 7" : "+v"(cs[0]), "+v"(cs[1]), "+v"(cs[2]), "+v"(cs[3]), "+v"(cs[4]), "+v"(cs[5]), "+v"(gram));
    float fv = (t >> 2) != g ? 0.f : (t & 3) == 0 ? gram[0] : (t & 3) == 1 ? gram[1] : (t & 3) == 2 ? gram[2] : gram[3];
    fv = rows_sum(row16_sum(fv));
    csv0 = cs[4][0]; csv1 = cs[5][0];
    wait_vm<0>();
    BAR();
    float cn[8];
#pragma unroll
    for (int i = 0; i < 8; ++i) cn[i] = 0.f;
#pragma unroll
    for (int ks = 0; ks < 4; ++ks) {
      v4u f[8];
#pragma unroll
      for (int tp = 0; tp < 4; ++tp) {
        const unsigned a = WO + (unsigned)ks * 8192u + (unsigned)(8 * g + q) * 256u + (unsigned)((((tp ^ q) << 2) | pp) << 4);
        tr_pair<0, 1024>(a, f[2 * tp], f[2 * tp + 1]);
      }
#pragma unroll
      for (int i = 0; i < 8; ++i) {
#pragma unroll
        for (int e = 0; e < 4; ++e) cn[i] = dot2(f[i][e], f[i][e], cn[i]);
        if (ks == 3 && i == 7) wo_last = f[i]; else sts128(WO + (unsigned)lane * 16u, ks * 8192 + i * 1024, f[i]);
      }
    }
    float cm = 0.f;
#pragma unroll
    for (int i = 0; i < 8; ++i) cm = fmaxf(cm, rows_sum(cn[i]));
    cm = row16_max(cm);
    BAR();
    if (t == 0) {
#pragma unroll
      for (int j = 0; j < 4; ++j) sts128f(MISC + (unsigned)g * 16u, j * 64, cs[j]);
    }
    if (lane == 0) sts64f(BNDW + wave * 8, 0, f32x2{fv, cm});
    if (wave == 0 && lane == 0) sts32(SIGW, 0, 0u);
    BAR();
    gu32* btab = (gu32*)(p.ws + OFF_BOUND) + (layer * 2 + rep) * 32;
    if (wave == 0 && lane == 0) {
      const f32x4 b0 = lds128f(BNDW), b1 = lds128f(BNDW, 16);
      const float f2 = (b0[0] + b0[2]) + (b1[0] + b1[2]), c2 = fmaxf(fmaxf(b0[1], b0[3]), fmaxf(b1[1], b1[3]));
      const float b = fmaxf(sqrtf(f2) * sqrtf(c2), 1e-30f);
      __hip_atomic_store(btab + h, __float_as_uint(b) | 1u, RLX_AGENT);
    }
  }
  STAMP(1);
  const rsrc_t rx = layer == 0 ? mkrsrc(p.x, 2048 * 1024) : mkrsrc(p.ws + OFF_X2, 2048 * 1024);
  bool have_bstat = false;
  bstat = 0.f;

  for (int s = rep; s < NSEG; s += 2) {
    const int lane = lane_id(), t = lane & 15, g = lane >> 4;
    pidx = s;
    STAMP(0); STAMPC(15);
    if (layer == 1) { if (!wait_ge((gu32*)(p.ws + OFF_CNTX2 + s * 128), 8u, abortw)) return; }
    STAMP(1);
    {
      const int row = 8 * wave + (lane >> 3), c8 = lane & 7;
      const unsigned xw = XT + (unsigned)row * 1024u + (unsigned)(((c8 ^ row) & 15) << 4);
      v4u xr[8];
#pragma unroll
      for (int i = 0; i < 8; ++i) xr[i] = ld_sc1(rx, row * 1024 + c8 * 16 + i * 128, s * 32768);
      float s1 = 0.f, s2 = 0.f;
#pragma unroll
      for (int i = 0; i < 8; ++i) {
#pragma unroll
        for (int e = 0; e < 4; ++e) { s1 = dot2(xr[i][e], ONES, s1); s2 = dot2(xr[i][e], xr[i][e], s2); }
        sts128((i & 1) ? (xw ^ 128u) : xw, (i >> 1) * 256, xr[i]);
      }
      s1 = oct_sum(s1); s2 = oct_sum(s2);
      const float mu = s1 * (1.f / 512.f);
      const float var = fmaxf(s2 * (1.f / 512.f) - mu * mu, 0.f);
      const float rs = __builtin_amdgcn_rsqf(var + 1e-5f);
      if (c8 == 0) sts64f(STATS + row * 8, 0, f32x2{rs, -rs * mu});
    }
    BAR();
    STAMP(2);
    v4u qf[2], kf[2], vf[2];
    {
      f32x4 aq[2][2], ak[2][2], av[2][2];
      const unsigned xb = XT + (unsigned)t * 1024u + (unsigned)(((t & 12) | ((g ^ t) & 3)) << 4);
      v4u xc[2], xn[2];
      xc[0] = lds128(xb); xc[1] = lds128(xb, 16384);
      sfor<16>([&](auto KK) {
        constexpr int kk = KK;
        if (kk < 15) {
          const unsigned a = xb ^ (unsigned)(((kk + 1) & 3) << 6);
          xn[0] = lds128(a, ((kk + 1) >> 2) * 256); xn[1] = lds128(a, ((kk + 1) >> 2) * 256 + 16384);
        }
        sfor<4>([&](auto I) {
          constexpr int m = I >> 1, j = I & 1;
          mfWA<SK + 16 * j + kk, kk == 0>(ak[m][j], xc[m]); mfWB<SV + 16 * j + kk, kk == 0>(av[m][j], xc[m]); mfWA<SQ + 16 * j + kk, kk == 0>(aq[m][j], xc[m]);
        });
        xc[0] = xn[0]; xc[1] = xn[1];
      });
      asm volatile("s_nop 7" : "+v"(aq[0][0]), "+v"(aq[0][1]), "+v"(aq[1][0]), "+v"(aq[1][1]), "+v"(ak[0][0]), "+v"(ak[0][1]), "+v"(ak[1][0]), "+v"(ak[1][1]),
                               "+v"(av[0][0]), "+v"(av[0][1]), "+v"(av[1][0]), "+v"(av[1][1]));
      f32x4 cq[2], ck[2];
      cq[0] = lds128f(MISC + g * 16); cq[1] = lds128f(MISC + g * 16, 64); ck[0] = lds128f(MISC + g * 16, 128); ck[1] = lds128f(MISC + g * 16, 192);
#pragma unroll
      for (int m = 0; m < 2; ++m) {
        const f32x2 st = lds64f(STATS + t * 8, m * 128);
#pragma unroll
        for (int j = 0; j < 2; ++j) {
          qf[m][2 * j] = cvtpk(st[0] * aq[m][j][0] + st[1] * cq[j][0], st[0] * aq[m][j][1] + st[1] * cq[j][1]);
          qf[m][2 * j + 1] = cvtpk(st[0] * aq[m][j][2] + st[1] * cq[j][2], st[0] * aq[m][j][3] + st[1] * cq[j][3]);
          kf[m][2 * j] = cvtpk(st[0] * ak[m][j][0] + st[1] * ck[j][0], st[0] * ak[m][j][1] + st[1] * ck[j][1]);
          kf[m][2 * j + 1] = cvtpk(st[0] * ak[m][j][2] + st[1] * ck[j][2], st[0] * ak[m][j][3] + st[1] * ck[j][3]);
        }
        const f32x4 sa = lds128f(STATS + g * 32, m * 128), sb = lds128f(STATS + g * 32, m * 128 + 16);
        vf[0][2 * m] = cvtpk(sa[0] * av[m][0][0] + sa[1] * csv0, sa[2] * av[m][0][1] + sa[3] * csv0);
        vf[0][2 * m + 1] = cvtpk(sb[0] * av[m][0][2] + sb[1] * csv0, sb[2] * av[m][0][3] + sb[3] * csv0);
        vf[1][2 * m] = cvtpk(sa[0] * av[m][1][0] + sa[1] * csv1, sa[2] * av[m][1][1] + sa[3] * csv1);
        vf[1][2 * m + 1] = cvtpk(sb[0] * av[m][1][2] + sb[1] * csv1, sb[2] * av[m][1][3] + sb[3] * csv1);
      }
    }
    asm volatile("" : "+v"(qf[0]), "+v"(qf[1]), "+v"(kf[0]), "+v"(kf[1]), "+v"(vf[0]), "+v"(vf[1]));
    if (!have_bstat) {
      gu32* btab = (gu32*)(p.ws + OFF_BOUND) + (layer * 2 + rep) * 32;
      for (unsigned spins = 0;;) {
        const unsigned v = __hip_atomic_load(btab + (lane & 31), RLX_AGENT);
        if (!__any(v == 0u)) { bstat = rows_max(row16_max(__uint_as_float(v))); break; }
        if (spin_fail(spins, abortw)) return;
        __builtin_amdgcn_s_sleep(1);
      }
      have_bstat = true;
    }
    BAR();
    STAMP(3);
    constexpr unsigned MEM = XT, SC = XT + 8192, OX = XT + 20480, ST = XT;
    float mmax = 0.f;
    f32x4 qm[2], km[2], vm[2];
    const unsigned mb = MEM + (unsigned)g * 128u + (unsigned)(t & 7) * 16u;
    {
      const int gk = lane >> 5, gg = (lane >> 3) & 3, gt = lane & 7;
      const unsigned mw = MEM + (unsigned)(4 * wave + gk) * 512u + (unsigned)gg * 128u + (unsigned)gt * 16u;
      float invS = 0.f;
      int abase = 0;
      if (s > 0) {
        unsigned hu;
        if (!wait_ge((gu32*)(p.ws + OFF_HU + ((layer * 2 + rep) * 32 + h) * 128), (unsigned)s, abortw, &hu, 0xffu)) return;
        invS = __uint_as_float((hu >> 8) << 23);
        abase = OFF_ACC + (s - 1) * 32768 + layer * 128;
      }
      STAMP(4);
      const int goff = ((4 * wave + gk) * 8 + gg) * 256 + gt * 16;
      bool dead = false;
      sfor<2>([&](auto PH) {
        constexpr int ph = PH;
        v4u fr;
        if (s > 0) {
          v4u c0, c1;
          for (unsigned spins = 0;;) {
            FENCE();
            c0 = ld_sc1(rws, goff, abase + ph * 4096);
            c1 = ld_sc1(rws, goff + 1024, abase + ph * 4096);
            const unsigned bad = ((c0.x ^ 0x60000000u) | (c0.z ^ 0x60000000u) | (c1.x ^ 0x60000000u) | (c1.z ^ 0x60000000u) |
                                  (c0.y ^ 0x80000000u) | (c0.w ^ 0x80000000u) | (c1.y ^ 0x80000000u) | (c1.w ^ 0x80000000u)) >> 28;
            if (!__any(bad != 0u)) break;
            if (spin_fail(spins, abortw)) { dead = true; break; }
            __builtin_amdgcn_s_sleep(BACKOFF);
          }
          float f[8];
          f[0] = (float)(int)(c0.x - 0x68000000u) * invS; f[1] = (float)(int)(c0.y - 0x88000009u) * invS;
          f[2] = (float)(int)(c1.x - 0x68000000u) * invS; f[3] = (float)(int)(c1.y - 0x88000009u) * invS;
          f[4] = (float)(int)(c0.z - 0x68000000u) * invS; f[5] = (float)(int)(c0.w - 0x88000009u) * invS;
          f[6] = (float)(int)(c1.z - 0x68000000u) * invS; f[7] = (float)(int)(c1.w - 0x88000009u) * invS;
          fr = v4u{cvtpk(f[0], f[1]), cvtpk(f[2], f[3]), cvtpk(f[4], f[5]), cvtpk(f[6], f[7])};
        } else {
          fr = *(const GAS v4u*)((const char*)p.mem0 + gt * 1024 + (4 * wave + 2 * ph + gk) * 64 + gg * 16);
        }
        sts128(mw, ph * 1024, fr);
#pragma unroll
        for (int e = 0; e < 4; ++e) mmax = fmaxf(mmax, fmaxf(fabsf(bflo(fr[e])), fabsf(bfhi(fr[e]))));
        BAR();
        if (ph == 0) STAMP(5);
        v4u mc = lds128(mb, (2 * ph) * 512), mn;
        sfor<8>([&](auto I) {
          constexpr int i = I, kk = 4 * (i >> 1) + 2 * ph + (i & 1);
          if (i < 7) { constexpr int k2 = 4 * ((i + 1) >> 1) + 2 * ph + ((i + 1) & 1); mn = lds128(mb, k2 * 512); }
          constexpr bool z = ph == 0 && i == 0;
          mfWA<SQ + kk, z>(qm[0], mc); mfWA<SK + kk, z>(km[0], mc); mfWA<SQ + 16 + kk, z>(qm[1], mc); mfWA<SK + 16 + kk, z>(km[1], mc);
          mc = mn;
        });
      });
      if (dead) return;
    }
    STAMP(6);
    mmax = rows_max(row16_max(mmax));
    if (lane == 0) sts32(MAXW + wave * 4, 0, __float_as_uint(mmax));
    asm volatile("s_nop 7" : "+v"(qm[0]), "+v"(qm[1]), "+v"(km[0]), "+v"(km[1]));
    v4u qmf = {cvtpk(qm[0][0], qm[0][1]), cvtpk(qm[0][2], qm[0][3]), cvtpk(qm[1][0], qm[1][1]), cvtpk(qm[1][2], qm[1][3])};
    v4u kmf = {cvtpk(km[0][0], km[0][1]), cvtpk(km[0][2], km[0][3]), cvtpk(km[1][0], km[1][1]), cvtpk(km[1][2], km[1][3])};
    v2u vmf[2];
    {
      f32x4 sc[3];
      asm volatile("s_nop 1" : "+v"(qmf), "+v"(kmf), "+v"(kf[0]), "+v"(kf[1]), "+v"(qf[0]), "+v"(qf[1]), "+v"(vf[0]), "+v"(vf[1]));
      MF32Z(sc[0], kmf, qmf); MF32Z(sc[1], kf[0], qmf); MF32Z(sc[2], kf[1], qmf);
      v4u mc = lds128(mb), mn;
#define VSTEP(kk) do { if ((kk) < 15) mn = lds128(mb, ((kk) + 1) * 512); \
        mfWB<SV + (kk), (kk) == 0>(vm[0], mc); mfWB<SV + 16 + (kk), (kk) == 0>(vm[1], mc); mc = mn; } while (0)
      VSTEP(0); VSTEP(1);
      asm volatile("s_nop 3" : "+v"(sc[0]), "+v"(sc[1]), "+v"(sc[2]));
#pragma unroll
      for (int i = 0; i < 3; ++i) sts128f(SC + (unsigned)wave * 3072u + (unsigned)lane * 16u, i * 1024, sc[i]);
      VSTEP(2); VSTEP(3);
      BAR();
      f32x4 pr[2][3];
      const unsigned sb = SC + (unsigned)(t >> 3) * 6144u + (unsigned)lane * 16u;
#pragma unroll
      for (int i = 0; i < 3; ++i) { pr[0][i] = lds128f(sb, i * 1024); pr[1][i] = lds128f(sb, 3072 + i * 1024); }
      const f32x4 mx4 = lds128f(MAXW);
      VSTEP(4); VSTEP(5); VSTEP(6); VSTEP(7);
      float sv[12];
#pragma unroll
      for (int i = 0; i < 3; ++i)
#pragma unroll
        for (int r = 0; r < 4; ++r) { const float u = pr[0][i][r] + pr[1][i][r]; sv[4 * i + r] = u + dpp<0x128>(u); }
      VSTEP(8); VSTEP(9);
      float m = sv[0];
#pragma unroll
      for (int i = 1; i < 12; ++i) m = fmaxf(m, sv[i]);
      m = rows_max(m);
      VSTEP(10); VSTEP(11);
      const float mc2 = m * SM_C;
      float ev[12], sum = 0.f;
#pragma unroll
      for (int i = 0; i < 12; ++i) ev[i] = __builtin_amdgcn_exp2f(sv[i] * SM_C - mc2);
      VSTEP(12); VSTEP(13);
      if (g >= 2) { ev[0] = 0.f; ev[1] = 0.f; ev[2] = 0.f; ev[3] = 0.f; }
      v2u pm = {cvtpk(ev[0], ev[1]), cvtpk(ev[2], ev[3])};
      v4u pt = {cvtpk(ev[4], ev[5]), cvtpk(ev[6], ev[7]), cvtpk(ev[8], ev[9]), cvtpk(ev[10], ev[11])};
      VSTEP(14); VSTEP(15);
#undef VSTEP
      const float xb = 25.f * fmaxf(1.f, fmaxf(fmaxf(mx4[0], mx4[1]), fmaxf(mx4[2], mx4[3]))) * bstat;
      int eb = (int)(__float_as_uint(xb) >> 23) + 1;
      eb = min(max(eb, 40), 220);
      const float encS = __uint_as_float((unsigned)(254 + 22 - eb) << 23);
      f32x4 oa[2];
#define SEND_HU() do { if (wave == 0 && lane == 0) \
        __hip_atomic_store((gu32*)(p.ws + OFF_HU + ((layer * 2 + (rep ^ 1)) * 32 + h) * 128), (unsigned)(s + 1) | ((unsigned)(eb - 22) << 8), RLX_AGENT); } while (0)
      if (HUPOS == -1) SEND_HU();
      asm volatile("s_nop 1" : "+v"(pm), "+v"(pt));
      MF32Z(oa[0], vf[0], pt); MF32Z(oa[1], vf[1], pt);
      asm volatile("s_nop 7" : "+v"(vm[0]), "+v"(vm[1]));
      vmf[0] = v2u{cvtpk(vm[0][0], vm[0][1]), cvtpk(vm[0][2], vm[0][3])};
      vmf[1] = v2u{cvtpk(vm[1][0], vm[1][1]), cvtpk(vm[1][2], vm[1][3])};
      asm volatile("s_nop 1" : "+v"(vmf[0]), "+v"(vmf[1]));
      {
        MF16(oa[0], vmf[0], pm); MF16(oa[1], vmf[1], pm);
#pragma unroll
        for (int i = 0; i < 12; ++i) sum += ev[i];
        const float inv = __builtin_amdgcn_rcpf(rows_sum(sum));
        asm volatile("s_nop 7" : "+v"(oa[0]), "+v"(oa[1]));
        const v4u of = {cvtpk(oa[0][0] * inv, oa[0][1] * inv), cvtpk(oa[0][2] * inv, oa[0][3] * inv),
                        cvtpk(oa[1][0] * inv, oa[1][1] * inv), cvtpk(oa[1][2] * inv, oa[1][3] * inv)};
        sts128(OX + (unsigned)wave * 1024u + (unsigned)lane * 16u, 0, of);
      }
      BAR();
      STAMP(7);
      if (s < NSEG - 1) {
        v4u ob[4];
#pragma unroll
        for (int i = 0; i < 4; ++i) ob[i] = lds128(lane * 16, OX + i * 1024);
        gu64* adst = (gu64*)(p.ws + OFF_ACC + s * 32768 + layer * 128 + (wave * 32 + g) * 256 + (t & 7) * 16 + (t >> 3) * 8);
        const float encS4 = encS * 4.f;
        const unsigned wb = WO + (unsigned)lane * 16u;
        f32x4 pa[2][2];
        v4u wc[2], wn[2];
        wc[0] = lds128(wb); wc[1] = lds128(wb, 1024);
#pragma unroll
        for (int i = 0; i < 5; ++i) {
          if (HUPOS == i) SEND_HU();
          if (i < 4) {
#pragma unroll
            for (int ks = 0; ks < 4; ++ks) {
              const int nx = i * 4 + ks + 1, ni = nx >> 2, nks = nx & 3;
              if (nx < 16) {
                wn[0] = lds128(wb, nks * 8192 + (2 * ni) * 1024);
                if (nks == 3 && ni == 3) wn[1] = wo_last; else wn[1] = lds128(wb, nks * 8192 + (2 * ni + 1) * 1024);
              }
              if (ks == 0) { MF32Z(pa[i & 1][0], wc[0], ob[ks]); MF32Z(pa[i & 1][1], wc[1], ob[ks]); }
              else { MF32(pa[i & 1][0], wc[0], ob[ks]); MF32(pa[i & 1][1], wc[1], ob[ks]); }
              wc[0] = wn[0]; wc[1] = wn[1];
            }
          }
          if (i > 0) {
            const int b = (i - 1) & 1;
            if (i == 4) asm volatile("s_nop 7" : "+v"(pa[b][0]), "+v"(pa[b][1])); else asm volatile("" : "+v"(pa[b][0]), "+v"(pa[b][1]));
            unsigned u[4];
#pragma unroll
            for (int r = 0; r < 4; ++r) {
              const float x = t < 8 ? pa[b][0][r] : pa[b][1][r];
              const unsigned bits = __float_as_uint((r & 1) ? fmaf(x, encS4, 50331648.f) : fmaf(x, encS, 12582912.f));
              u[r] = (r & 1) ? min(max(bits, 0x4C000000u), 0x4C7FFFF0u) : min(max(bits, 0x4B000000u), 0x4B7FFFF0u);
            }
            __hip_atomic_fetch_add(adst + (i - 1) * 256, ((unsigned long long)u[1] << 32) | u[0], RLX_AGENT);
            __hip_atomic_fetch_add(adst + (i - 1) * 256 + 128, ((unsigned long long)u[3] << 32) | u[2], RLX_AGENT);
          }
        }
        if (HUPOS == 5) SEND_HU();
      }
    }
    STAMP(8);
    {
      f32x4 sc[5];
      MF32Z(sc[0], kmf, qf[0]); MF32Z(sc[1], kf[0], qf[0]);
      MF32Z(sc[2], kmf, qf[1]); MF32Z(sc[3], kf[0], qf[1]); MF32Z(sc[4], kf[1], qf[1]);
      asm volatile("s_nop 7" : "+v"(sc[0]), "+v"(sc[1]), "+v"(sc[2]), "+v"(sc[3]), "+v"(sc[4]));
#pragma unroll
      for (int i = 0; i < 5; ++i) sts128f((unsigned)wave * 5120u + (unsigned)lane * 16u, ST + i * 1024, sc[i]);
      BAR();
#pragma unroll
      for (int i = 0; i < 5; ++i) {
        const unsigned a = (unsigned)lane * 16u;
        const int o = ST + i * 1024;
        sc[i] = (lds128f(a, o) + lds128f(a, o + 5120)) + (lds128f(a, o + 10240) + lds128f(a, o + 15360));
      }
      BAR();
      constexpr float NEG = -1.0e9f / 0.08838834764831845f;
#pragma unroll
      for (int r = 0; r < 4; ++r) { if (4 * g + r > t) { sc[1][r] = NEG; sc[4][r] = NEG; } }
      float m0 = fmaxf(fmaxf(fmaxf(sc[0][0], sc[0][1]), fmaxf(sc[0][2], sc[0][3])), fmaxf(fmaxf(sc[1][0], sc[1][1]), fmaxf(sc[1][2], sc[1][3])));
      float m1 = fmaxf(fmaxf(fmaxf(sc[2][0], sc[2][1]), fmaxf(sc[2][2], sc[2][3])), fmaxf(fmaxf(sc[3][0], sc[3][1]), fmaxf(sc[3][2], sc[3][3])));
      m1 = fmaxf(m1, fmaxf(fmaxf(sc[4][0], sc[4][1]), fmaxf(sc[4][2], sc[4][3])));
      m0 = rows_max(m0) * SM_C; m1 = rows_max(m1) * SM_C;
      float s0 = 0.f, s1 = 0.f;
#pragma unroll
      for (int i = 0; i < 5; ++i)
#pragma unroll
        for (int r = 0; r < 4; ++r) {
          float e = __builtin_amdgcn_exp2f(sc[i][r] * SM_C - (i < 2 ? m0 : m1));
          if ((i == 0 || i == 2) && g >= 2) e = 0.f;
          sc[i][r] = e;
          if (i < 2) s0 += e; else s1 += e;
        }
      v2u pm0 = {cvtpk(sc[0][0], sc[0][1]), cvtpk(sc[0][2], sc[0][3])}, pm1 = {cvtpk(sc[2][0], sc[2][1]), cvtpk(sc[2][2], sc[2][3])};
      v4u pt0 = {cvtpk(sc[1][0], sc[1][1]), cvtpk(sc[1][2], sc[1][3]), 0u, 0u};
      v4u pt1 = {cvtpk(sc[3][0], sc[3][1]), cvtpk(sc[3][2], sc[3][3]), cvtpk(sc[4][0], sc[4][1]), cvtpk(sc[4][2], sc[4][3])};
      f32x4 oa[2][2];
      asm volatile("s_nop 1" : "+v"(pm0), "+v"(pm1), "+v"(pt0), "+v"(pt1));
      MF32Z(oa[0][0], vf[0], pt0); MF32Z(oa[0][1], vf[1], pt0); MF32Z(oa[1][0], vf[0], pt1); MF32Z(oa[1][1], vf[1], pt1);
      MF16(oa[0][0], vmf[0], pm0); MF16(oa[0][1], vmf[1], pm0); MF16(oa[1][0], vmf[0], pm1); MF16(oa[1][1], vmf[1], pm1);
      const float i0 = __builtin_amdgcn_rcpf(rows_sum(s0)), i1 = __builtin_amdgcn_rcpf(rows_sum(s1));
      asm volatile("s_nop 7" : "+v"(oa[0][0]), "+v"(oa[0][1]), "+v"(oa[1][0]), "+v"(oa[1][1]));
#pragma unroll
      for (int m = 0; m < 2; ++m) {
        const float iv = m ? i1 : i0;
        const v4u of = {cvtpk(oa[m][0][0] * iv, oa[m][0][1] * iv), cvtpk(oa[m][0][2] * iv, oa[m][0][3] * iv),
                        cvtpk(oa[m][1][0] * iv, oa[m][1][1] * iv), cvtpk(oa[m][1][2] * iv, oa[m][1][3] * iv)};
        st_sc1(of, rws, lane * 16, OFF_O + ((layer * NUNIT + 2 * s + m) * 128 + 4 * h + wave) * 1024);
      }
    }
    STAMP(9);
    DRAIN();
    signal_last(SIGW, (gu32*)(p.ws + OFF_CNTO + ((layer * 64 + s) * 4 + (h >> 3)) * 128), lane);
    STAMP(10);
  }
}

template <int KS, int NT, bool ISW>
DI void role_WD(const Params& p, const int role, const int wave, const int layer, const int rep, const int nrep, const int cb) {
  constexpr int RB = NT * 32;
  constexpr int KB = 32 * RB;
  constexpr int IPK = KB / 1024;
  constexpr int KR = 96 / NT;
  constexpr int KL = KS - KR;
  constexpr int NSLOT = 40960 / KB;
  constexpr int DEPTH = KS < 16 ? KS : 16;
  const int lane = lane_id();
  const int g = lane >> 4;
  gu32* abortw = (gu32*)(p.ws + OFF_ABORT);
  const rsrc_t rws = mkrsrc(p.ws, WS_USED);
  int pidx = 127; (void)pidx;
  STAMP(0);
  const unsigned WL = (unsigned)wave * (unsigned)(KL * KB);
  constexpr unsigned RED = 4 * KL * KB;
  {
    const char* src = (const char*)(ISW ? p.wo : p.wd) + ((size_t)wave * KS * 32 * 512 + cb * NT * 16) * 2;
    const int q = (lane >> 2) & 3, pp = lane & 3;
    constexpr int RPI = 1024 / RB, LPR = RB / 16;
    const int lrow = lane / LPR, lch = lane % LPR;
    const int sw = RB == 256 ? ((lrow & 3) << 2) : (((lrow >> 1) & 1) << 2);
    const unsigned voff = (unsigned)lrow * 1024u + (unsigned)((lch ^ sw) << 4);
    const int qs = RB == 256 ? q : (q >> 1);
    const unsigned ring = (unsigned)wave * 40960u;
    auto issue = [&](int ks, unsigned dst) {
#pragma unroll
      for (int i = 0; i < IPK; ++i) glds16(src + (size_t)(ks * 32 + i * RPI) * 1024, voff, dst + (unsigned)i * 1024u);
    };
    auto fetch = [&](unsigned base, v4u (&f)[NT]) {
#pragma unroll
      for (int tp = 0; tp < NT / 2; ++tp)
        tr_pair<0, 4 * RB>(base + (unsigned)(8 * g + q) * (unsigned)RB + (unsigned)((((tp ^ qs) << 2) | pp) << 4), f[2 * tp], f[2 * tp + 1]);
    };
    constexpr int PRE = NSLOT < KR ? NSLOT : KR;
#pragma unroll
    for (int ks = 0; ks < PRE; ++ks) issue(ks, ring + (unsigned)ks * KB);
    sfor<KR>([&](auto KSI) {
      constexpr int ks = KSI;
      wait_vm<((ks + PRE <= KR ? PRE : KR - ks) - 1) * IPK>();
      sfor<NT>([&](auto N) {
        constexpr int n = N;
        tr_slot<ks * NT + n, (ks % NSLOT) * KB + 8 * (n & 1), 4 * RB>(ring + (unsigned)(8 * g + q) * (unsigned)RB + (unsigned)(((((n >> 1) ^ qs) << 2) | pp) << 4));
      });
      WAIT_LDS();
      if (ks + PRE < KR) issue(ks + PRE, ring + (unsigned)(ks % NSLOT) * KB);
    });
    BAR();
#pragma unroll
    for (int i = 0; i < KL; ++i) issue(KR + i, WL + (unsigned)i * KB);
    wait_vm<0>();
#pragma unroll
    for (int i = 0; i < KL; ++i) {
      v4u f[NT];
      fetch(WL + (unsigned)i * KB, f);
#pragma unroll
      for (int n = 0; n < NT; ++n) sts128(WL + (unsigned)lane * 16u, i * KB + n * 1024, f[n]);
    }
    BAR();
  }
  STAMP(1);
  const rsrc_t rres = ISW ? (layer == 0 ? mkrsrc(p.x, 2048 * 1024) : mkrsrc(p.ws + OFF_X2, 2048 * 1024)) : mkrsrc(p.ws + OFF_X1 + layer * (2 << 20), 2048 * 1024);
  const rsrc_t rdst = ISW ? mkrsrc(p.ws + OFF_X1 + layer * (2 << 20), 2048 * 1024) : (layer == 0 ? mkrsrc(p.ws + OFF_X2, 2048 * 1024) : mkrsrc(p.out, 2048 * 1024));
  constexpr int LPRO = NT * 2;
  const int er = lane / LPRO, ec = lane % LPRO;
  const bool eact = er < 4;
  const int erow = 4 * wave + (er & 3);
  const int eoff = erow * 1024 + cb * RB + ec * 16;
  for (int u = rep; u < NUNIT; u += nrep) {
    pidx = u;
    STAMP(0);
    if (ISW) { if (!wait_ge((gu32*)(p.ws + OFF_CNTO + ((layer * 64 + (u >> 1)) * 4 + wave) * 128), 8u, abortw)) return; }
    else { if (!wait_ge((gu32*)(p.ws + OFF_CNTH + (layer * NUNIT + u) * 128), 14u, abortw)) return; }
    STAMP(1);
    const int abase = (ISW ? OFF_O + (layer * NUNIT + u) * 131072 : OFF_HB + (layer * NUNIT + u) * 57344) + wave * KS * 1024;
    v4u ar[DEPTH];
#pragma unroll
    for (int i = 0; i < DEPTH; ++i) ar[i] = ld_sc1(rws, lane * 16, abase + i * 1024);
    f32x4 acc[NT];
    sfor<KS>([&](auto KSI) {
      constexpr int ks = KSI;
      if constexpr (ks < KR) {
        sfor<NT>([&](auto N) { constexpr int n = N; mfWA<ks * NT + n, ks == 0>(acc[n], ar[ks % DEPTH]); });
      } else {
        v4u wl[NT];
#pragma unroll
        for (int n = 0; n < NT; ++n) wl[n] = lds128(WL + (unsigned)lane * 16u, (ks - KR) * KB + n * 1024);
#pragma unroll
        for (int n = 0; n < NT; ++n) MF32(acc[n], wl[n], ar[ks % DEPTH]);
      }
      if (ks + DEPTH < KS) ar[ks % DEPTH] = ld_sc1(rws, lane * 16, abase + (ks + DEPTH) * 1024);
    });
    const v4u res = ld_sc1(rres, eoff, u * 16384);
    STAMP(2);
    asm volatile("s_nop 7" ::: "memory");
    constexpr unsigned rb = RED;
#pragma unroll
    for (int n = 0; n < NT; ++n) {
      asm volatile("" : "+v"(acc[n]));
      sts128f(rb + (unsigned)(wave * NT) * 1024u + (unsigned)lane * 16u, n * 1024, acc[n]);
    }
    BAR();
    {
      const unsigned a = rb + (unsigned)(ec >> 2) * 2048u + (unsigned)((ec & 3) * 16 + erow) * 16u;
      f32x4 lo = (lds128f(a) + lds128f(a, NT * 1024)) + (lds128f(a, 2 * NT * 1024) + lds128f(a, 3 * NT * 1024));
      f32x4 hi = (lds128f(a, 1024) + lds128f(a, NT * 1024 + 1024)) + (lds128f(a, 2 * NT * 1024 + 1024) + lds128f(a, 3 * NT * 1024 + 1024));
      const v4u y = {cvtpk(lo[0], lo[1]), cvtpk(lo[2], lo[3]), cvtpk(hi[0], hi[1]), cvtpk(hi[2], hi[3])};
      v4u o;
#pragma unroll
      for (int e = 0; e < 4; ++e) o[e] = cvtpk(bflo(res[e]) + bflo(y[e]), bfhi(res[e]) + bfhi(y[e]));
      if (eact) st_sc1(o, rdst, eoff, u * 16384);
    }
    STAMP(3);
    DRAIN();
    BAR();
    if (wave == 0 && lane == 0) {
      if (ISW) __hip_atomic_fetch_add((gu32*)(p.ws + OFF_CNTX1 + (layer * NUNIT + u) * 128), 1u, RLX_AGENT);
      else if (layer == 0) __hip_atomic_fetch_add((gu32*)(p.ws + OFF_CNTX2 + (u >> 1) * 128), 1u, RLX_AGENT);
    }
    STAMP(4);
  }
}

DI void role_G(const Params& p, const int role, const int wave, const int layer, const int rep, const int gi) {
  const int lane = lane_id();
  const int t = lane & 15, g = lane >> 4;
  gu32* abortw = (gu32*)(p.ws + OFF_ABORT);
  const rsrc_t rws = mkrsrc(p.ws, WS_USED);
  int pidx = 127; (void)pidx;
  STAMP(0);
  constexpr int SG = 0, SU = 32;
  f32x4 cs[4];
  constexpr unsigned STATS = 65536, SIGW = 66560;
  {
    auto src_of = [&](int c) -> const char* {
      return (const char*)(c < 4 ? p.wg : p.wu) + ((size_t)(128 * (c & 3)) * FF + gi * 128) * 2;
    };
    const int q = (lane >> 2) & 3, pp = lane & 3;
    const unsigned trb = (unsigned)(8 * g + q) * 256u + (unsigned)(((4 * wave + pp) ^ (q << 2)) << 4);
#pragma unroll
    for (int c = 0; c < 4; ++c) chunk_issue(src_of(c), FF * 2, (unsigned)c * 32768u, wave, lane);
    sfor<8>([&](auto C) {
      constexpr int c = C;
      wait_vm<(c <= 4 ? 24 : 8 * (7 - c))>();
      BAR();
      if (c + 4 < 8) chunk_issue(src_of(c + 4), FF * 2, (unsigned)((c + 4) % 5) * 32768u, wave, lane);
      tr_quad<(c < 4 ? SG : SU), 4 * (c & 3)>(trb + (unsigned)(c % 5) * 32768u);
    });
    v4u ones = {ONES, ONES, ONES, ONES};
    asm volatile("s_nop 1" : "+v"(ones));
    sfor<32>([&](auto I) {
      constexpr int kk = I >> 1, j = I & 1;
      mfWA<SG + 16 * j + kk, kk == 0>(cs[j], ones); mfWA<SU + 16 * j + kk, kk == 0>(cs[2 + j], ones);
    });
    asm volatile("s_nop 7" : "+v"(cs[0]), "+v"(cs[1]), "+v"(cs[2]), "+v"(cs[3]));
    BAR();
    if (wave == 0 && lane == 0) sts32(SIGW, 0, 0u);
    BAR();
  }
  STAMP(1);
  const rsrc_t rx = mkrsrc(p.ws + OFF_X1 + layer * (2 << 20), 2048 * 1024);
  int par = 0;
  for (int u = rep; u < NUNIT; u += 2, par ^= 1) {
    pidx = u;
    STAMP(0);
    if (!wait_ge((gu32*)(p.ws + OFF_CNTX1 + (layer * NUNIT + u) * 128), 8u, abortw)) return;
    STAMP(1);
    const unsigned XT = (unsigned)par * 16384u, stb = STATS + (unsigned)par * 128u;
    {
      const int row = 4 * wave + g;
      v4u xr[4];
#pragma unroll
      for (int i = 0; i < 4; ++i) xr[i] = ld_sc1(rx, row * 1024 + t * 16 + i * 256, u * 16384);
      float s1 = 0.f, s2 = 0.f;
#pragma unroll
      for (int i = 0; i < 4; ++i) {
#pragma unroll
        for (int e = 0; e < 4; ++e) { s1 = dot2(xr[i][e], ONES, s1); s2 = dot2(xr[i][e], xr[i][e], s2); }
        sts128(XT + (unsigned)row * 1024u + (unsigned)(((t ^ row) & 15) << 4), i * 256, xr[i]);
      }
      s1 = row16_sum(s1); s2 = row16_sum(s2);
      const float mu = s1 * (1.f / 512.f);
      const float var = fmaxf(s2 * (1.f / 512.f) - mu * mu, 0.f);
      const float rs = __builtin_amdgcn_rsqf(var + 1e-5f);
      if (t == 0) sts64f(stb + row * 8, 0, f32x2{rs, -rs * mu});
    }
    BAR();
    STAMP(2);
    f32x4 ag[2], au[2];
    const unsigned xb = XT + (unsigned)t * 1024u + (unsigned)(((t & 12) | ((g ^ t) & 3)) << 4);
    v4u xf[16];
#pragma unroll
    for (int kk = 0; kk < 16; ++kk) xf[kk] = lds128(xb ^ (unsigned)((kk & 3) << 6), (kk >> 2) * 256);
    const f32x2 st = lds64f(stb + t * 8);
    sfor<32>([&](auto I) {
      constexpr int kk = I >> 1, j = I & 1;
      mfWA<SG + 16 * j + kk, kk == 0>(ag[j], xf[kk]); mfWA<SU + 16 * j + kk, kk == 0>(au[j], xf[kk]);
    });
    asm volatile("s_nop 7" : "+v"(ag[0]), "+v"(ag[1]), "+v"(au[0]), "+v"(au[1]));
    v4u hf;
#pragma unroll
    for (int j = 0; j < 2; ++j)
#pragma unroll
      for (int r = 0; r < 4; r += 2) {
        const unsigned gp = cvtpk(st[0] * ag[j][r] + st[1] * cs[j][r], st[0] * ag[j][r + 1] + st[1] * cs[j][r + 1]);
        const unsigned up = cvtpk(st[0] * au[j][r] + st[1] * cs[2 + j][r], st[0] * au[j][r + 1] + st[1] * cs[2 + j][r + 1]);
        const float g0 = bflo(gp), g1 = bfhi(gp);
        hf[2 * j + (r >> 1)] = cvtpk((g0 > 0.f ? g0 : g0 * 0.f) * bflo(up), (g1 > 0.f ? g1 : g1 * 0.f) * bfhi(up));
      }
    st_sc1(hf, rws, lane * 16, OFF_HB + (layer * NUNIT + u) * 57344 + (4 * gi + wave) * 1024);
    STAMP(3);
    DRAIN();
    signal_last(SIGW, (gu32*)(p.ws + OFF_CNTH + (layer * NUNIT + u) * 128), lane);
    STAMP(4);
  }
}

__global__ void __launch_bounds__(256, 1) __attribute__((amdgpu_num_vgpr(128))) rmt_reglu_kernel(Params p) {
  asm volatile("" ::: "v255", "a255");
  extern __shared__ __attribute__((aligned(16))) unsigned char lds_dyn[];
  if (threadIdx.x == 0) {
    unsigned x = (unsigned)__builtin_amdgcn_s_getreg(20 | (3 << 11)) & 7u, r = 0;
    for (int i = 0; i < 8; ++i) {
      const unsigned tk = __hip_atomic_fetch_add((gu32*)(p.ws + OFF_TICKET + x * 256), 1u, RLX_AGENT);
      if (tk < 32u) { r = x * 32u + tk; break; }
      x = (x + 1) & 7u;
    }
    *(LAS unsigned*)lds_dyn = r;
  }
  BAR();
  const int role = rfl(*(LAS unsigned*)lds_dyn);
  BAR();
  const int wave = rfl(threadIdx.x >> 6);
  const int layer = role >> 7, xq = (role >> 5) & 3, sl = role & 31;
  const int k = sl - 16;
  if (sl < 16) role_H(p, role, wave, layer, sl >> 3, xq * 8 + (sl & 7));
  else if (xq == 0 || (xq == 1 && k < 8)) role_WD<32, 4, true>(p, role, wave, layer, xq * 2 + (k >> 3), 3, k & 7);
  else if (xq >= 2 && k < 14) role_G(p, role, wave, layer, xq - 2, k);
  else role_WD<14, 8, false>(p, role, wave, layer, xq == 1 ? (k - 8) >> 2 : 2, 3, xq == 1 ? (k & 3) : (xq - 2) * 2 + (k - 14));
}

extern "C" void kernel_launch(void* const* d_in, const int* in_sizes, int n_in, void* d_out, int out_size, void* d_ws, size_t ws_size,
                              hipStream_t stream) {
  (void)in_sizes; (void)n_in; (void)out_size; (void)ws_size;
  Params p;
  p.x = d_in[0]; p.mem0 = d_in[1]; p.wq = d_in[2]; p.wk = d_in[3]; p.wv = d_in[4]; p.wo = d_in[5];
  p.wg = d_in[8]; p.wu = d_in[9]; p.wd = d_in[10];
  p.out = d_out; p.ws = (char*)d_ws;
  (void)hipFuncSetAttribute((const void*)rmt_reglu_kernel, hipFuncAttributeMaxDynamicSharedMemorySize, LDS_BYTES);
  (void)hipMemsetAsync(d_ws, 0, CTL_BYTES, stream);
  hipLaunchKernelGGL(rmt_reglu_kernel, dim3(256), dim3(256), LDS_BYTES, stream, p);
}
```

```cpp
#include <hip/hip_runtime.h>
#include <stdint.h>
#include <utility>

#define LAS __attribute__((address_space(3)))
#define GAS __attribute__((address_space(1)))
#define DI __device__ __forceinline__
#define RLX_AGENT __ATOMIC_RELAXED, __HIP_MEMORY_SCOPE_AGENT
typedef unsigned v4u __attribute__((ext_vector_type(4)));
typedef unsigned v2u __attribute__((ext_vector_type(2)));
typedef float f32x4 __attribute__((ext_vector_type(4)));
typedef float f32x2 __attribute__((ext_vector_type(2)));
typedef __bf16 bf16x2 __attribute__((ext_vector_type(2)));
typedef GAS unsigned gu32;
typedef GAS unsigned long long gu64;
typedef __amdgpu_buffer_rsrc_t rsrc_t;

constexpr int NSEG = 64, NUNIT = 128, FF = 1792;
constexpr int LDS_BYTES = 163840;
constexpr int OFF_ABORT = 2048;
constexpr int OFF_BOUND = 4096;
constexpr int OFF_HU = 8192;
constexpr int PST = 256;
constexpr int OFF_ZERO = 24576;
constexpr int OFF_PH = 32768;
constexpr int OFF_PW = OFF_PH + 128 * PST;
constexpr int OFF_PG = OFF_PW + 48 * PST;
constexpr int OFF_PD = OFF_PG + 64 * PST;
constexpr int OFF_CNTS = OFF_PD + 16 * PST;
constexpr int OFF_ACC = 2 << 20;
constexpr int CTL_BYTES = OFF_CNTS + PST;
constexpr int OFF_X1 = 4 << 20;
constexpr int OFF_X2 = 8 << 20;
constexpr int OFF_ST1 = 12 << 20;
constexpr int OFF_ST2 = 13 << 20;
constexpr int OFF_O = 16 << 20;
constexpr int OFF_HB = 48 << 20;
constexpr int OFF_PROF = 96 << 20;
constexpr int WS_USED = 120 << 20;
static_assert(OFF_CNTS + PST <= OFF_ACC && OFF_HU / 32 + 3840 == OFF_BOUND && PST == 256 && OFF_ACC == (2 << 20) && OFF_ST2 == (13 << 20) && OFF_ZERO == 24576 && OFF_HU == 8192, "constants the generated code relies on");
constexpr int OOB = 0x7ff00000;
#define ROTK 0
#define HOLD0 600
#define PSLEEP 1
constexpr unsigned SPIN_CAP = 1u << 18;

struct Params {
  const void *x, *mem0, *wq, *wk, *wv, *wo, *wg, *wu, *wd;
  void* out;
  char* ws;
};

#define HPROFCLOB
#define STAMP(slot) do {} while (0)
#define STAMPR() do {} while (0)
#define FLUSH() do {} while (0)

DI unsigned rfl(unsigned x) { return __builtin_amdgcn_readfirstlane(x); }
DI unsigned f2u(float x) { return __builtin_bit_cast(unsigned, x); }
DI float u2f(unsigned x) { return __builtin_bit_cast(float, x); }
DI unsigned cvtpk(float lo, float hi) { f32x2 v = {lo, hi}; bf16x2 b = __builtin_convertvector(v, bf16x2); return __builtin_bit_cast(unsigned, b); }
DI float bflo(unsigned w) { return u2f(w << 16); }
DI float bfhi(unsigned w) { return u2f(w & 0xffff0000u); }
DI float fma_(float a, float b, float c) { return __builtin_fmaf(a, b, c); }
DI float max2(float a, float b) { float r; asm("v_max_f32_e32 %0, %1, %2" : "=v"(r) : "v"(a), "v"(b)); return r; }
DI float max3(float a, float b, float c) { float r; asm("v_max3_f32 %0, %1, %2, %3" : "=v"(r) : "v"(a), "v"(b), "v"(c)); return r; }
DI float amax3(float a, float b, float c) { float r; asm("v_max3_f32 %0, |%1|, |%2|, %3" : "=v"(r) : "v"(a), "v"(b), "v"(c)); return r; }
DI unsigned umed3(unsigned a, unsigned lo, unsigned hi) { return a < lo ? lo : a > hi ? hi : a; }
DI bool any_lane(bool c) { return __builtin_amdgcn_ballot_w64(c) != 0ull; }
DI float dot2(unsigned a, unsigned b, float c) { return __builtin_amdgcn_fdot2_f32_bf16(__builtin_bit_cast(bf16x2, a), __builtin_bit_cast(bf16x2, b), c, false); }
template <int CTRL> DI float dpp(float v) { return __builtin_bit_cast(float, __builtin_amdgcn_update_dpp(0, __builtin_bit_cast(int, v), CTRL, 0xf, 0xf, true)); }
DI float rows_max(float v) {
  auto a = __builtin_amdgcn_permlane16_swap(f2u(v), f2u(v), false, false);
  v = max2(u2f(a[0]), u2f(a[1]));
  auto b = __builtin_amdgcn_permlane32_swap(f2u(v), f2u(v), false, false);
  return max2(u2f(b[0]), u2f(b[1]));
}
DI float half_sum(float v) { auto b = __builtin_amdgcn_permlane32_swap(f2u(v), f2u(v), false, false); return u2f(b[0]) + u2f(b[1]); }
DI float rows_sum(float v) { auto a = __builtin_amdgcn_permlane16_swap(f2u(v), f2u(v), false, false); return half_sum(u2f(a[0]) + u2f(a[1])); }
DI float row16_sum(float v) { v += dpp<0x128>(v); v += dpp<0x124>(v); v += dpp<0x122>(v); v += dpp<0x121>(v); return v; }
DI float row16_max(float v) { v = max2(v, dpp<0x128>(v)); v = max2(v, dpp<0x124>(v)); v = max2(v, dpp<0x122>(v)); v = max2(v, dpp<0x121>(v)); return v; }
DI float oct_sum(float v) { v += dpp<0xB1>(v); v += dpp<0x4E>(v); v += dpp<0x141>(v); return v; }
DI f32x2 ln_coef(float s1, float s2) {
  const float mu = s1 * (1.f / 512.f);
  const float var = max2(fma_(-mu, mu, s2 * (1.f / 512.f)), 0.f);
  const float rs = __builtin_amdgcn_rsqf(var + 1e-5f);
  return f32x2{rs, -rs * mu};
}

DI LAS char* lptr(unsigned a) { return (LAS char*)(uintptr_t)a; }
DI v4u lds128(unsigned a, int o = 0) { return *(const LAS v4u*)(lptr(a) + o); }
DI f32x4 lds128f(unsigned a, int o = 0) { return *(const LAS f32x4*)(lptr(a) + o); }
DI f32x2 lds64f(unsigned a, int o = 0) { return *(const LAS f32x2*)(lptr(a) + o); }
DI void sts128(unsigned a, int o, v4u v) { *(LAS v4u*)(lptr(a) + o) = v; }
DI void sts128f(unsigned a, int o, f32x4 v) { *(LAS f32x4*)(lptr(a) + o) = v; }
DI void sts64f(unsigned a, int o, f32x2 v) { *(LAS f32x2*)(lptr(a) + o) = v; }
DI float lds32f(unsigned a, int o = 0) { return *(const LAS float*)(lptr(a) + o); }
DI void sts32(unsigned a, int o, unsigned v) { *(LAS unsigned*)(lptr(a) + o) = v; }
DI int lane_id() { int l; asm volatile("v_mbcnt_lo_u32_b32 %0, -1, 0\n\tv_mbcnt_hi_u32_b32 %0, -1, %0" : "=v"(l)); __builtin_assume(l >= 0 && l < 64); return l; }
DI rsrc_t mkrsrc(const void* ptr, unsigned bytes) { return __builtin_amdgcn_make_buffer_rsrc((void*)ptr, 0, bytes, 0x00020000); }
DI v4u ld_sc1(rsrc_t r, int voff, int soff) { return __builtin_amdgcn_raw_buffer_load_b128(r, voff, soff, 16); }
DI unsigned ld32_sc1(rsrc_t r, int voff, int soff) { return __builtin_amdgcn_raw_buffer_load_b32(r, voff, soff, 16); }
DI void st32_sc1(unsigned v, rsrc_t r, int voff, int soff) { __builtin_amdgcn_raw_buffer_store_b32(v, r, voff, soff, 16); }
DI void st_sc1(v4u v, rsrc_t r, int voff, int soff) { __builtin_amdgcn_raw_buffer_store_b128(v, r, voff, soff, 16); }
#define ACLOB "a0","a1","a2","a3","a4","a5","a6","a7","a8","a9","a10","a11","a12","a13","a14","a15","a16","a17","a18","a19","a20","a21","a22","a23","a24","a25","a26","a27","a28","a29","a30","a31","a32","a33","a34","a35","a36","a37","a38","a39","a40","a41","a42","a43","a44","a45","a46","a47","a48","a49","a50","a51","a52","a53","a54","a55","a56","a57","a58","a59","a60","a61","a62","a63","a64","a65","a66","a67","a68","a69","a70","a71","a72","a73","a74","a75","a76","a77","a78","a79","a80","a81","a82","a83","a84","a85","a86","a87","a88","a89","a90","a91","a92","a93","a94","a95","a96","a97","a98","a99","a100","a101","a102","a103","a104","a105","a106","a107","a108","a109","a110","a111","a112","a113","a114","a115","a116","a117","a118","a119","a120","a121","a122","a123","a124","a125","a126","a127"
#define BAR() asm volatile("s_waitcnt lgkmcnt(0)\n\ts_barrier" ::: "memory", ACLOB)
#define DRAIN() asm volatile("s_waitcnt vmcnt(0)" ::: "memory")
#define FENCE() asm volatile("" ::: "memory")
#define GAP() do { asm volatile("" ::: ACLOB); __builtin_amdgcn_sched_barrier(0); } while (0)
#define SB() __builtin_amdgcn_sched_barrier(0)
#define PIN(x) asm volatile("" : "+v"(x))
#define UNDEF(x) asm volatile("" : "=v"(x))

template <int... Is, class F> DI void sfor_(std::integer_sequence<int, Is...>, F&& f) { (f(std::integral_constant<int, Is>{}), ...); }
template <int N> using IC = std::integral_constant<int, N>;
template <int N, class F> DI void sfor(F&& f) { sfor_(std::make_integer_sequence<int, N>{}, f); }

#define MF32(acc, A, B)  asm volatile("v_mfma_f32_16x16x32_bf16 %0, %1, %2, %0" : "+v"(acc) : "v"(A), "v"(B))
#define MF32Z(acc, A, B) asm volatile("v_mfma_f32_16x16x32_bf16 %0, %1, %2, 0" : "=&v"(acc) : "v"(A), "v"(B))
#define MF16Z(acc, A, B) asm volatile("v_mfma_f32_16x16x16_bf16 %0, %1, %2, 0" : "=&v"(acc) : "v"(A), "v"(B))
#define MF16(acc, A, B)  asm volatile("v_mfma_f32_16x16x16_bf16 %0, %1, %2, %0" : "+v"(acc) : "v"(A), "v"(B))
constexpr int slot_reg(int slot) { return slot < 64 ? 4 * slot : 128 + 4 * (slot - 64); }
template <int SLOT, bool Z> DI void mfWA(f32x4& acc, const v4u& x) {
  constexpr int R = slot_reg(SLOT);
  if constexpr (SLOT < 64) {
    if constexpr (Z) asm volatile("v_mfma_f32_16x16x32_bf16 %0, a[%c2:%c3], %1, 0" : "=&v"(acc) : "v"(x), "i"(R), "i"(R + 3));
    else asm volatile("v_mfma_f32_16x16x32_bf16 %0, a[%c2:%c3], %1, %0" : "+v"(acc) : "v"(x), "i"(R), "i"(R + 3));
  } else {
    if constexpr (Z) asm volatile("v_mfma_f32_16x16x32_bf16 %0, v[%c2:%c3], %1, 0" : "=&v"(acc) : "v"(x), "i"(R), "i"(R + 3));
    else asm volatile("v_mfma_f32_16x16x32_bf16 %0, v[%c2:%c3], %1, %0" : "+v"(acc) : "v"(x), "i"(R), "i"(R + 3));
  }
}
template <int SLOT, bool Z> DI void mfWB(f32x4& acc, const v4u& x) {
  static_assert(SLOT >= 64, "");
  constexpr int R = slot_reg(SLOT);
  if constexpr (Z) asm volatile("v_mfma_f32_16x16x32_bf16 %0, %1, v[%c2:%c3], 0" : "=&v"(acc) : "v"(x), "i"(R), "i"(R + 3));
  else asm volatile("v_mfma_f32_16x16x32_bf16 %0, %1, v[%c2:%c3], %0" : "+v"(acc) : "v"(x), "i"(R), "i"(R + 3));
}
template <int SLOT, bool Z> DI void mfWW(f32x4& acc) {
  constexpr int R = slot_reg(SLOT);
  if constexpr (Z) asm volatile("v_mfma_f32_16x16x32_bf16 %0, v[%c1:%c2], v[%c1:%c2], 0" : "=&v"(acc) : "i"(R), "i"(R + 3));
  else asm volatile("v_mfma_f32_16x16x32_bf16 %0, v[%c1:%c2], v[%c1:%c2], %0" : "+v"(acc) : "i"(R), "i"(R + 3));
}
template <int SLOT, int OFF, int HS> DI void tr_slot(unsigned a) {
  constexpr int R = slot_reg(SLOT);
  if constexpr (SLOT < 64)
    asm volatile("ds_read_b64_tr_b16 a[%c1:%c2], %0 offset:%c5\n\tds_read_b64_tr_b16 a[%c3:%c4], %0 offset:%c6"
                 :: "v"(a), "i"(R), "i"(R + 1), "i"(R + 2), "i"(R + 3), "i"(OFF), "i"(OFF + HS) : "memory");
  else
    asm volatile("ds_read_b64_tr_b16 v[%c1:%c2], %0 offset:%c5\n\tds_read_b64_tr_b16 v[%c3:%c4], %0 offset:%c6"
                 :: "v"(a), "i"(R), "i"(R + 1), "i"(R + 2), "i"(R + 3), "i"(OFF), "i"(OFF + HS) : "memory");
}
#define WAIT_LDS() asm volatile("s_waitcnt lgkmcnt(0)" ::: "memory")

DI void spin_check(unsigned& spins, gu32* abortw) {
  unsigned t0, t1;
  asm volatile("s_add_u32 %0, %0, 1\n\ts_cmp_lt_u32 %0, %4\n\ts_cbranch_scc1 .Lspin_ok_%=\n\t"
               "v_mov_b32 %1, 0\n\tv_mov_b32 %2, 1\n\tglobal_store_dword %1, %2, %3 sc1\n\ts_endpgm\n.Lspin_ok_%=:"
               : "+s"(spins), "=&v"(t0), "=&v"(t1) : "s"(abortw), "i"(SPIN_CAP) : "scc", "memory");
}
DI void wait_prog(rsrc_t rws, int off, int n, unsigned want, gu32* abortw) {
  const int l = lane_id() & 15, voff = (l < n ? l : 0) * PST;
  for (unsigned spins = 0;;) {
    FENCE();
    const unsigned v = ld32_sc1(rws, voff, off);
    if (!any_lane(v < want)) { FENCE(); return; }
    spin_check(spins, abortw);
    __builtin_amdgcn_s_sleep(PSLEEP);
  }
}
DI void count_add(rsrc_t rws, int off, unsigned n) {
  asm volatile("s_mov_b64 exec, 1\n\tbuffer_atomic_add %0, off, %1, %2\n\ts_mov_b64 exec, -1" :: "v"(n), "s"(rws), "s"(off) : "memory");
}
DI unsigned arrive_issue(unsigned ldsword) {
  unsigned old;
  asm volatile("s_mov_b64 exec, 1\n\tds_add_rtn_u32 %0, %1, %2\n\ts_mov_b64 exec, -1" : "=&v"(old) : "v"(ldsword), "v"(1u) : "memory");
  return old;
}
DI bool arrive_last(unsigned old) {
  unsigned r;
  asm volatile("s_waitcnt lgkmcnt(0)\n\tv_readfirstlane_b32 %0, %1" : "=s"(r) : "v"(old) : "memory");
  return (r & 3u) == 3u;
}
DI void signal_last(unsigned ldsword, rsrc_t rws, int off, unsigned v) { if (arrive_last(arrive_issue(ldsword))) st32_sc1(v, rws, 0, off); }

DI void glds16(const void* sbase, unsigned voff, unsigned dst) {
  unsigned keep;
  asm volatile("s_mov_b32 %0, m0\n\ts_mov_b32 m0, %3\n\ts_nop 0\n\tglobal_load_lds_dwordx4 %1, %2\n\ts_mov_b32 m0, %0"
               : "=&s"(keep) : "v"(voff), "s"(sbase), "s"(dst) : "memory");
}
template <int N> DI void wait_vm() { asm volatile("s_waitcnt vmcnt(%0)" :: "i"(N) : "memory"); }
template <int OFF, int HS> DI void tr_pair(unsigned a, v4u& f0, v4u& f1) {
  v2u r0, r1, r2, r3;
  asm volatile("ds_read_b64_tr_b16 %0, %4 offset:%5\n\tds_read_b64_tr_b16 %1, %4 offset:%6\n\t"
               "ds_read_b64_tr_b16 %2, %4 offset:%7\n\tds_read_b64_tr_b16 %3, %4 offset:%8\n\ts_waitcnt lgkmcnt(0)"
               : "=&v"(r0), "=&v"(r1), "=&v"(r2), "=&v"(r3) : "v"(a), "i"(OFF), "i"(OFF + HS), "i"(OFF + 8), "i"(OFF + HS + 8) : "memory");
  f0 = v4u{r0.x, r0.y, r1.x, r1.y};
  f1 = v4u{r2.x, r2.y, r3.x, r3.y};
}
template <int OFF, int HS> DI void tr_oct(const unsigned (&a)[4], v4u (&f)[8]) {
  v2u r[16];
  asm volatile("ds_read_b64_tr_b16 %0, %16 offset:%20\n\tds_read_b64_tr_b16 %1, %16 offset:%21\n\tds_read_b64_tr_b16 %2, %16 offset:%22\n\tds_read_b64_tr_b16 %3, %16 offset:%23\n\t"
               "ds_read_b64_tr_b16 %4, %17 offset:%20\n\tds_read_b64_tr_b16 %5, %17 offset:%21\n\tds_read_b64_tr_b16 %6, %17 offset:%22\n\tds_read_b64_tr_b16 %7, %17 offset:%23\n\t"
               "ds_read_b64_tr_b16 %8, %18 offset:%20\n\tds_read_b64_tr_b16 %9, %18 offset:%21\n\tds_read_b64_tr_b16 %10, %18 offset:%22\n\tds_read_b64_tr_b16 %11, %18 offset:%23\n\t"
               "ds_read_b64_tr_b16 %12, %19 offset:%20\n\tds_read_b64_tr_b16 %13, %19 offset:%21\n\tds_read_b64_tr_b16 %14, %19 offset:%22\n\tds_read_b64_tr_b16 %15, %19 offset:%23\n\t"
               "s_waitcnt lgkmcnt(0)"
               : "=&v"(r[0]), "=&v"(r[1]), "=&v"(r[2]), "=&v"(r[3]), "=&v"(r[4]), "=&v"(r[5]), "=&v"(r[6]), "=&v"(r[7]),
                 "=&v"(r[8]), "=&v"(r[9]), "=&v"(r[10]), "=&v"(r[11]), "=&v"(r[12]), "=&v"(r[13]), "=&v"(r[14]), "=&v"(r[15])
               : "v"(a[0]), "v"(a[1]), "v"(a[2]), "v"(a[3]), "i"(OFF), "i"(OFF + HS), "i"(OFF + 8), "i"(OFF + HS + 8) : "memory");
#pragma unroll
  for (int i = 0; i < 8; ++i) f[i] = v4u{r[2 * i].x, r[2 * i].y, r[2 * i + 1].x, r[2 * i + 1].y};
  asm volatile("s_nop 1" : "+v"(f[0]), "+v"(f[1]), "+v"(f[2]), "+v"(f[3]), "+v"(f[4]), "+v"(f[5]), "+v"(f[6]), "+v"(f[7]));
}
template <int S0, int K0> DI void tr_quad(unsigned a) {
  sfor<8>([&](auto I) { constexpr int i = I >> 1, j = I & 1; tr_slot<S0 + 16 * j + K0 + i, 8192 * i + 8 * j, 1024>(a); });
  WAIT_LDS();
}
DI void chunk_issue(const char* src, unsigned ldb, unsigned slot, int wave, int lane) {
  const unsigned voff = (unsigned)(lane >> 4) * ldb + (unsigned)(((lane & 15) ^ ((lane >> 4) << 2)) << 4);
#pragma unroll
  for (int i = 0; i < 8; ++i) glds16(src + (size_t)(32 * wave + 4 * i) * ldb, voff, slot + (unsigned)(32 * wave + 4 * i) * 256u);
}

constexpr float SM_C = 0.08838834764831845f * 1.4426950408889634f;
constexpr unsigned ONES = 0x3f803f80u;

DI void role_H(const Params& p, const int role, const int wave, const int layer, const int rep, const int h) {
  const int lane = lane_id();
  const int t = lane & 15, g = lane >> 4;
  gu32* abortw = (gu32*)(p.ws + OFF_ABORT);
  const rsrc_t rws = mkrsrc(p.ws, WS_USED);
  int pidx = 128; (void)pidx;
  unsigned pv = 0; (void)pv;
  STAMPR(); STAMP(0);
  constexpr int SQ = 0, SK = 32, SV = 64;
  v4u wo_last;
    const unsigned WO = (unsigned)((wave + 2) % 5) * 32768u;
  const unsigned MISC = WO + 31744;
  constexpr unsigned MISC1 = 3 * 32768 + 31744;
  constexpr unsigned SIGW = MISC1 + 784, BNDW = MISC1 + 800;
  float csv0, csv1;
  {
    const int rot = ROTK * (layer * 2 + rep);
    auto issue = [&](int it) {
      int c = it + rot; c -= c >= 12 ? 12 : 0;
      const char* base = (const char*)(c < 4 ? p.wk : c < 8 ? p.wv : p.wq);
      const char* src = it < 12 ? base + ((size_t)(128 * (c & 3)) * 4096 + h * 128) * 2 : (const char*)p.wo + ((size_t)(h * 128) * 512 + (it - 12) * 128) * 2;
      chunk_issue(src, it < 12 ? 8192u : 1024u, (unsigned)(it % 5) * 32768u, wave, lane);
    };
    const int q = (lane >> 2) & 3, pp = lane & 3;
    const unsigned trb = (unsigned)(8 * g + q) * 256u + (unsigned)(((4 * wave + pp) ^ (q << 2)) << 4);
#pragma unroll 1
    for (int it = 0; it < 4; ++it) issue(it);
    v4u ones = {ONES, ONES, ONES, ONES};
    f32x4 cs[6], gram = {0.f, 0.f, 0.f, 0.f};
#pragma unroll
    for (int i = 0; i < 6; ++i) cs[i] = gram;
#pragma unroll 1
    for (int it = 0; it < 12; ++it) {
      wait_vm<24>();
      BAR();
      if (it == 0) STAMP(4);
      if (it == 3) STAMP(5);
      if (it == 7) STAMP(6);
      issue(it + 4);
      int cc = it + rot; cc -= cc >= 12 ? 12 : 0;
      const unsigned ta = trb + (unsigned)(it % 5) * 32768u;
      asm volatile("s_nop 1" : "+v"(ones), "+v"(cs[0]), "+v"(cs[1]), "+v"(cs[2]), "+v"(cs[3]), "+v"(cs[4]), "+v"(cs[5]), "+v"(gram));
      sfor<12>([&](auto C) {
        constexpr int c = C;
        if (cc == c) {
          tr_quad<(c < 4 ? SK : c < 8 ? SV : SQ), 4 * (c & 3)>(ta);
          sfor<8>([&](auto I) {
            constexpr int kk = 4 * (c & 3) + (I >> 1), j = I & 1;
            if constexpr (c < 4) mfWA<SK + 16 * j + kk, false>(cs[2 + j], ones);
            else if constexpr (c < 8) { mfWB<SV + 16 * j + kk, false>(cs[4 + j], ones); mfWW<SV + 16 * j + kk, false>(gram); }
            else mfWA<SQ + 16 * j + kk, false>(cs[j], ones);
          });
          asm volatile("s_nop 7" : "+v"(cs[0]), "+v"(cs[1]), "+v"(cs[2]), "+v"(cs[3]), "+v"(cs[4]), "+v"(cs[5]), "+v"(gram));
        }
      });
    }
    STAMP(2);
    asm volatile("s_nop 7" : "+v"(cs[0]), "+v"(cs[1]), "+v"(cs[2]), "+v"(cs[3]), "+v"(cs[4]), "+v"(cs[5]), "+v"(gram));
    float fv = (t >> 2) != g ? 0.f : (t & 3) == 0 ? gram[0] : (t & 3) == 1 ? gram[1] : (t & 3) == 2 ? gram[2] : gram[3];
    fv = rows_sum(row16_sum(fv));
    csv0 = cs[4][0]; csv1 = cs[5][0];
    PIN(csv0); PIN(csv1);
    wait_vm<0>();
    BAR();
    STAMP(3);
    f32x4 cn[8];
    unsigned ta[4];
#pragma unroll
    for (int tp = 0; tp < 4; ++tp)
      ta[tp] = WO + (unsigned)(8 * g + q) * 256u + (unsigned)((((tp ^ q ^ (h & 3)) << 2) | pp) << 4);
    sfor<4>([&](auto KS) {
      constexpr int ks = KS;
      v4u f[8];
      tr_oct<ks * 8192, 1024>(ta, f);
#pragma unroll
      for (int i = 0; i < 8; ++i) {
        if (ks == 0) MF32Z(cn[i], f[i], f[i]); else MF32(cn[i], f[i], f[i]);
        if (ks == 3 && i == 7) wo_last = f[i]; else sts128(WO + (unsigned)lane * 16u, ks * 8192 + i * 1024, f[i]);
      }
    });
    asm volatile("s_nop 7" : "+v"(cn[0]), "+v"(cn[1]), "+v"(cn[2]), "+v"(cn[3]), "+v"(cn[4]), "+v"(cn[5]), "+v"(cn[6]), "+v"(cn[7]));
    float cm = 0.f;
#pragma unroll
    for (int i = 0; i < 8; ++i) cm = max3(max3(cm, cn[i][0], cn[i][1]), cn[i][2], cn[i][3]);
    cm = rows_max(row16_max(cm));
    BAR();
#pragma unroll
    for (int j = 0; j < 4; ++j) sts128f(MISC + (unsigned)g * 16u, j * 64, cs[j]);
    sts64f(BNDW + wave * 8, 0, f32x2{fv, cm});
    sts32(SIGW, 0, 0u);
    BAR();
    if (wave == 0) {
      const f32x4 b0 = lds128f(BNDW), b1 = lds128f(BNDW, 16);
      const float f2 = (b0[0] + b0[2]) + (b1[0] + b1[2]), c2 = max2(max2(b0[1], b0[3]), max2(b1[1], b1[3]));
      const float b = max2(__builtin_amdgcn_sqrtf(f2) * __builtin_amdgcn_sqrtf(c2), 1e-30f);
      st32_sc1(f2u(b) | 1u, rws, 0, OFF_BOUND + (layer * 2 + rep) * 128 + h * 4);
    }
  }
  STAMP(1); FLUSH();
  const rsrc_t rx = layer == 0 ? mkrsrc(p.x, 2048 * 1024) : mkrsrc(p.ws + OFF_X2, 2048 * 1024);
  const int grp = layer * 2 + rep;
  asm volatile(
  "v_mbcnt_lo_u32_b32 v0, -1, 0\n"
  "v_mbcnt_hi_u32_b32 v0, -1, v0\n"
  "v_and_b32 v1, 15, v0\n"
  "v_lshrrev_b32 v2, 4, v0\n"
  "v_lshlrev_b32 v104, 4, v0\n"
  "s_lshl_b32 s67, %[wave], 10\n"
  "v_add_u32 v105, s67, v104\n"
  "s_lshl_b32 s67, %[wave], 6\n"
  "v_xor_b32 v106, s67, v104\n"
  "v_and_b32 v4, 7, v0\n"
  "v_lshlrev_b32 v5, 4, v4\n"
  "v_xor_b32 v6, v2, v1\n"
  "v_and_b32 v6, 3, v6\n"
  "v_and_or_b32 v6, v1, 12, v6\n"
  "v_lshlrev_b32 v6, 4, v6\n"
  "v_lshl_add_u32 v108, v1, 10, v6\n"
  "v_xor_b32 v109, 64, v108\n"
  "v_xor_b32 v110, 128, v108\n"
  "v_xor_b32 v111, 192, v108\n"
  "v_lshrrev_b32 v7, 5, v0\n"
  "v_bfe_u32 v6, v0, 3, 1\n"
  "v_lshl_add_u32 v6, v7, 1, v6\n"
  "v_bfe_u32 v7, v0, 4, 1\n"
  "v_lshl_add_u32 v6, v6, 1, v7\n"
  "v_lshl_add_u32 v6, v6, 9, v5\n"
  "s_lshl_b32 s67, %[wave], 13\n"
  "v_add_u32 v114, s67, v6\n"
  "v_lshl_add_u32 v115, v2, 7, v5\n"
  "s_lshl_b32 s68, %[wave], 2\n"
  "v_add_u32 v116, s68, v104\n"
  "v_add_u32 v117, %[wo], v104\n"
  "s_add_u32 s68, %[wo], 31744\n"
  "v_lshlrev_b32 v7, 4, v2\n"
  "v_add_u32 v118, s68, v7\n"
  "v_lshrrev_b32 v6, 3, v1\n"
  "v_lshl_add_u32 v7, v2, 8, v5\n"
  "v_lshl_add_u32 v7, v6, 3, v7\n"
  "v_add_u32 v119, s67, v7\n"
  "v_lshlrev_b32 v122, 3, v104\n"
  "v_and_b32 v122, 0xf80, v122\n"
  "v_mov_b32 v123, 0x1ff00\n"
  "s_mov_b32 s76, 0xff00ff\n"
  "s_mov_b32 s77, 0xff00ff\n"
  "s_mov_b32 s78, 0xffffffff\n"
  "s_mov_b32 s79, 0x0\n"
  "v_mov_b32 v6, 0xeeee\n"
  "v_mov_b32 v7, 0x4444\n"
  "v_cndmask_b32_e64 v107, v6, v7, s[76:77]\n"
  "s_lshl_b32 s68, %[wave], 10\n"
  "v_lshrrev_b32 v6, 2, v1\n"
  "v_lshrrev_b32 v7, 1, v2\n"
  "v_lshl_add_u32 v6, v6, 1, v7\n"
  "v_and_b32 v7, 3, v1\n"
  "v_lshl_add_u32 v6, v6, 2, v7\n"
  "v_and_b32 v7, 1, v2\n"
  "v_lshl_add_u32 v6, v6, 1, v7\n"
  "v_lshl_add_u32 v112, v6, 3, s68\n"
  "v_bfe_u32 v6, v0, 2, 2\n"
  "v_lshl_add_u32 v6, v2, 3, v6\n"
  "v_and_b32 v7, 1, v0\n"
  "v_lshl_add_u32 v6, v6, 1, v7\n"
  "v_lshl_add_u32 v113, v6, 3, s68\n"
  "s_mov_b32 s80, 0xf0000\n"
  "s_mov_b32 s81, 0xfff00ff\n"
  "s_mov_b32 s82, 0x1f0001\n"
  "s_mov_b32 s83, 0x1fff01ff\n"
  "s_mov_b32 s84, 0x3f0003\n"
  "s_mov_b32 s85, 0x3fff03ff\n"
  "s_mov_b32 s86, 0x7f0007\n"
  "s_mov_b32 s87, 0x7fff07ff\n"
  "s_mov_b32 s94, 0x3e0293ee\n"
  "s_mov_b32 s95, 0x3e0293ee\n"
  "s_mov_b32 s61, 0x0\n"
  "s_mov_b32 s62, 0x0\n"
  "s_mov_b32 s70, 0x0\n"
  "s_mov_b32 s90, 0x0\n"
  "s_mov_b32 s27, 0x96\n"
  "s_mov_b32 s28, 0x800\n"
  ".Lh_top_%=:\n"
  "s_cmp_eq_u32 s61, 0\n"
  "s_cbranch_scc1 .Lh_nox_%=\n"
  "s_waitcnt vmcnt(2)\n"
  ".Lh_x_%=:\n"
  "s_barrier\n"
  "ds_read_b128 v[36:39], v108 offset:49152\n"
  "ds_read_b128 v[32:35], v108 offset:32768\n"
  "ds_read_b128 v[40:43], v109 offset:32768\n"
  "ds_read_b128 v[44:47], v109 offset:49152\n"
  "s_waitcnt lgkmcnt(3)\n"
  "v_mfma_f32_16x16x32_bf16 v[16:19], v[36:39], v[128:131], 0\n"
  "ds_read_b128 v[68:71], v110 offset:32768\n"
  "ds_read_b128 v[72:75], v110 offset:49152\n"
  "v_mfma_f32_16x16x32_bf16 v[20:23], v[36:39], v[192:195], 0\n"
  "v_lshrrev_b32 v64, 4, v122\n"
  "s_waitcnt lgkmcnt(4)\n"
  "v_mfma_f32_16x16x32_bf16 v[24:27], v[32:35], v[128:131], 0\n"
  "v_and_b32 v64, 0x78, v64\n"
  "v_and_or_b32 v64, v122, s28, v64\n"
  "v_mfma_f32_16x16x32_bf16 v[28:31], v[32:35], v[192:195], 0\n"
  "v_lshlrev_b32 v65, 1, v104\n"
  "s_waitcnt lgkmcnt(2)\n"
  "v_mfma_f32_16x16x32_bf16 v[16:19], v[44:47], v[132:135], v[16:19]\n"
  "ds_read_b128 v[32:35], v111 offset:32768\n"
  "ds_read_b128 v[36:39], v111 offset:49152\n"
  "v_mfma_f32_16x16x32_bf16 v[20:23], v[44:47], v[196:199], v[20:23]\n"
  "v_and_b32 v65, 0x400, v65\n"
  "v_add_u32 v64, v64, v65\n"
  "v_mfma_f32_16x16x32_bf16 v[24:27], v[40:43], v[132:135], v[24:27]\n"
  "s_lshl_b32 s67, s60, 2\n"
  "s_add_u32 s67, s67, %[xstr]\n"
  "v_mfma_f32_16x16x32_bf16 v[28:31], v[40:43], v[196:199], v[28:31]\n"
  "s_lshl_b32 s67, s67, 10\n"
  "s_waitcnt lgkmcnt(2)\n"
  "v_mfma_f32_16x16x32_bf16 v[16:19], v[72:75], v[136:139], v[16:19]\n"
  "ds_read_b128 v[40:43], v108 offset:33024\n"
  "ds_read_b128 v[44:47], v108 offset:49408\n"
  "v_mfma_f32_16x16x32_bf16 v[20:23], v[72:75], v[200:203], v[20:23]\n"
  "s_add_u32 s67, s67, 0xd00000\n"
  "s_add_u32 s58, s60, 3\n"
  "v_mfma_f32_16x16x32_bf16 v[24:27], v[68:71], v[136:139], v[24:27]\n"
  "s_lshl_b32 s68, s58, 1\n"
  "s_mul_i32 s59, s68, 0x5556\n"
  "v_mfma_f32_16x16x32_bf16 v[28:31], v[68:71], v[200:203], v[28:31]\n"
  "buffer_load_dwordx2 v[76:77], v64, %[rws], s67 offen offset:0 sc1\n"
  "s_waitcnt lgkmcnt(2)\n"
  "v_mfma_f32_16x16x32_bf16 v[16:19], v[36:39], v[140:143], v[16:19]\n"
  "ds_read_b128 v[68:71], v109 offset:33024\n"
  "ds_read_b128 v[72:75], v109 offset:49408\n"
  "v_mfma_f32_16x16x32_bf16 v[20:23], v[36:39], v[204:207], v[20:23]\n"
  "buffer_load_dwordx2 v[78:79], v64, %[rws], s67 offen offset:128 sc1\n"
  "s_lshr_b32 s59, s59, 16\n"
  "v_mfma_f32_16x16x32_bf16 v[24:27], v[32:35], v[140:143], v[24:27]\n"
  "s_mul_i32 s59, s59, 3\n"
  "s_sub_u32 s68, s68, s59\n"
  "v_mfma_f32_16x16x32_bf16 v[28:31], v[32:35], v[204:207], v[28:31]\n"
  "buffer_load_dwordx2 v[80:81], v64, %[rws], s67 offen offset:256 sc1\n"
  "s_waitcnt lgkmcnt(2)\n"
  "v_mfma_f32_16x16x32_bf16 v[16:19], v[44:47], v[144:147], v[16:19]\n"
  "ds_read_b128 v[32:35], v110 offset:33024\n"
  "ds_read_b128 v[36:39], v110 offset:49408\n"
  "v_mfma_f32_16x16x32_bf16 v[20:23], v[44:47], v[208:211], v[20:23]\n"
  "buffer_load_dwordx2 v[82:83], v64, %[rws], s67 offen offset:384 sc1\n"
  "s_lshl_b32 s68, s68, 2\n"
  "v_mfma_f32_16x16x32_bf16 v[24:27], v[40:43], v[144:147], v[24:27]\n"
  "s_lshl_b32 s59, 15, s68\n"
  "s_xor_b32 s59, s59, 0xfff\n"
  "v_mfma_f32_16x16x32_bf16 v[28:31], v[40:43], v[208:211], v[28:31]\n"
  "buffer_load_dwordx2 v[84:85], v64, %[rws], s67 offen offset:512 sc1\n"
  "s_waitcnt lgkmcnt(2)\n"
  "v_mfma_f32_16x16x32_bf16 v[16:19], v[72:75], v[148:151], v[16:19]\n"
  "ds_read_b128 v[40:43], v111 offset:33024\n"
  "ds_read_b128 v[44:47], v111 offset:49408\n"
  "v_mfma_f32_16x16x32_bf16 v[20:23], v[72:75], v[212:215], v[20:23]\n"
  "buffer_load_dwordx2 v[86:87], v64, %[rws], s67 offen offset:640 sc1\n"
  "s_lshr_b32 s68, %[xstr], 2\n"
  "v_mfma_f32_16x16x32_bf16 v[24:27], v[68:71], v[148:151], v[24:27]\n"
  "s_sub_u32 s68, 64, s68\n"
  "s_max_u32 s58, s58, s68\n"
  "v_mfma_f32_16x16x32_bf16 v[28:31], v[68:71], v[212:215], v[28:31]\n"
  "buffer_load_dwordx2 v[96:97], v64, %[rws], s67 offen offset:768 sc1\n"
  "s_waitcnt lgkmcnt(2)\n"
  "v_mfma_f32_16x16x32_bf16 v[16:19], v[36:39], v[152:155], v[16:19]\n"
  "ds_read_b128 v[68:71], v108 offset:33280\n"
  "ds_read_b128 v[72:75], v108 offset:49664\n"
  "v_mfma_f32_16x16x32_bf16 v[20:23], v[36:39], v[216:219], v[20:23]\n"
  "buffer_load_dwordx2 v[98:99], v64, %[rws], s67 offen offset:896 sc1\n"
  "s_add_u32 s68, s60, 2\n"
  "v_mfma_f32_16x16x32_bf16 v[24:27], v[32:35], v[152:155], v[24:27]\n"
  "s_lshl_b32 s92, s68, 15\n"
  "s_lshl_b32 s93, %[wave], 12\n"
  "v_mfma_f32_16x16x32_bf16 v[28:31], v[32:35], v[216:219], v[28:31]\n"
  "s_add_u32 s92, s92, s93\n"
  "s_waitcnt lgkmcnt(2)\n"
  "v_mfma_f32_16x16x32_bf16 v[16:19], v[44:47], v[156:159], v[16:19]\n"
  "ds_read_b128 v[32:35], v109 offset:33280\n"
  "ds_read_b128 v[36:39], v109 offset:49664\n"
  "v_mfma_f32_16x16x32_bf16 v[20:23], v[44:47], v[220:223], v[20:23]\n"
  "s_add_u32 s93, s93, 32768\n"
  "v_mfma_f32_16x16x32_bf16 v[24:27], v[40:43], v[156:159], v[24:27]\n"
  "v_mfma_f32_16x16x32_bf16 v[28:31], v[40:43], v[220:223], v[28:31]\n"
  "s_waitcnt lgkmcnt(2)\n"
  "v_mfma_f32_16x16x32_bf16 v[16:19], v[72:75], v[160:163], v[16:19]\n"
  "ds_read_b128 v[40:43], v110 offset:33280\n"
  "ds_read_b128 v[44:47], v110 offset:49664\n"
  "v_mfma_f32_16x16x32_bf16 v[20:23], v[72:75], v[224:227], v[20:23]\n"
  "v_mfma_f32_16x16x32_bf16 v[24:27], v[68:71], v[160:163], v[24:27]\n"
  "v_mfma_f32_16x16x32_bf16 v[28:31], v[68:71], v[224:227], v[28:31]\n"
  "s_waitcnt lgkmcnt(2)\n"
  "v_mfma_f32_16x16x32_bf16 v[16:19], v[36:39], v[164:167], v[16:19]\n"
  "ds_read_b128 v[68:71], v111 offset:33280\n"
  "ds_read_b128 v[72:75], v111 offset:49664\n"
  "v_mfma_f32_16x16x32_bf16 v[20:23], v[36:39], v[228:231], v[20:23]\n"
  "v_mfma_f32_16x16x32_bf16 v[24:27], v[32:35], v[164:167], v[24:27]\n"
  "v_mfma_f32_16x16x32_bf16 v[28:31], v[32:35], v[228:231], v[28:31]\n"
  "s_waitcnt lgkmcnt(2)\n"
  "v_mfma_f32_16x16x32_bf16 v[16:19], v[44:47], v[168:171], v[16:19]\n"
  "ds_read_b128 v[32:35], v108 offset:33536\n"
  "ds_read_b128 v[36:39], v108 offset:49920\n"
  "v_mfma_f32_16x16x32_bf16 v[20:23], v[44:47], v[232:235], v[20:23]\n"
  "s_waitcnt vmcnt(8)\n"
  "v_mov_b32 v101, s62\n"
  "v_mfma_f32_16x16x32_bf16 v[24:27], v[40:43], v[168:171], v[24:27]\n"
  "s_mov_b64 exec, 1\n"
  "ds_add_rtn_u32 v100, v123, v101 offset:16\n"
  "s_mov_b64 exec, -1\n"
  "v_mfma_f32_16x16x32_bf16 v[28:31], v[40:43], v[232:235], v[28:31]\n"
  "s_waitcnt lgkmcnt(3)\n"
  "v_mfma_f32_16x16x32_bf16 v[16:19], v[72:75], v[172:175], v[16:19]\n"
  "ds_read_b128 v[40:43], v109 offset:33536\n"
  "ds_read_b128 v[44:47], v109 offset:49920\n"
  "v_mfma_f32_16x16x32_bf16 v[20:23], v[72:75], v[236:239], v[20:23]\n"
  "v_mfma_f32_16x16x32_bf16 v[24:27], v[68:71], v[172:175], v[24:27]\n"
  "v_mfma_f32_16x16x32_bf16 v[28:31], v[68:71], v[236:239], v[28:31]\n"
  "s_waitcnt lgkmcnt(3)\n"
  "v_mfma_f32_16x16x32_bf16 v[16:19], v[36:39], v[176:179], v[16:19]\n"
  "ds_read_b128 v[68:71], v110 offset:33536\n"
  "ds_read_b128 v[72:75], v110 offset:49920\n"
  "v_mfma_f32_16x16x32_bf16 v[20:23], v[36:39], v[240:243], v[20:23]\n"
  "s_waitcnt lgkmcnt(4)\n"
  "v_readfirstlane_b32 s67, v100\n"
  "v_mfma_f32_16x16x32_bf16 v[24:27], v[32:35], v[176:179], v[24:27]\n"
  "v_mov_b32 v101, s90\n"
  "s_and_b32 s67, s67, 3\n"
  "v_mfma_f32_16x16x32_bf16 v[28:31], v[32:35], v[240:243], v[28:31]\n"
  "s_add_u32 s67, s67, s62\n"
  "s_waitcnt lgkmcnt(2)\n"
  "v_mfma_f32_16x16x32_bf16 v[16:19], v[44:47], v[180:183], v[16:19]\n"
  "ds_read_b128 v[32:35], v111 offset:33536\n"
  "ds_read_b128 v[36:39], v111 offset:49920\n"
  "v_mfma_f32_16x16x32_bf16 v[20:23], v[44:47], v[244:247], v[20:23]\n"
  "s_add_u32 s68, %[cnto], 128\n"
  "s_cmp_eq_u32 s67, 4\n"
  "v_mfma_f32_16x16x32_bf16 v[24:27], v[40:43], v[180:183], v[24:27]\n"
  "s_cselect_b32 s67, %[cnto], s68\n"
  "buffer_store_dword v101, off, %[rws], s67 sc1\n"
  "v_mfma_f32_16x16x32_bf16 v[28:31], v[40:43], v[244:247], v[28:31]\n"
  "s_waitcnt lgkmcnt(2)\n"
  "v_mfma_f32_16x16x32_bf16 v[16:19], v[72:75], v[184:187], v[16:19]\n"
  "ds_read_b128 v[40:43], v108 offset:32768\n"
  "ds_read_b128 v[44:47], v108 offset:49152\n"
  "v_mfma_f32_16x16x32_bf16 v[20:23], v[72:75], v[248:251], v[20:23]\n"
  "v_mfma_f32_16x16x32_bf16 v[24:27], v[68:71], v[184:187], v[24:27]\n"
  "v_mfma_f32_16x16x32_bf16 v[28:31], v[68:71], v[248:251], v[28:31]\n"
  "s_waitcnt lgkmcnt(2)\n"
  "v_mfma_f32_16x16x32_bf16 v[16:19], v[36:39], v[188:191], v[16:19]\n"
  "ds_read_b128 v[68:71], v109 offset:32768\n"
  "ds_read_b128 v[72:75], v109 offset:49152\n"
  "v_mfma_f32_16x16x32_bf16 v[20:23], v[36:39], v[252:255], v[20:23]\n"
  "v_mfma_f32_16x16x32_bf16 v[24:27], v[32:35], v[188:191], v[24:27]\n"
  "v_mfma_f32_16x16x32_bf16 v[28:31], v[32:35], v[252:255], v[28:31]\n"
  "s_waitcnt lgkmcnt(2)\n"
  "v_mfma_f32_16x16x32_bf16 v[8:11], a[128:131], v[44:47], 0\n"
  "ds_read_b128 v[32:35], v110 offset:32768\n"
  "ds_read_b128 v[36:39], v110 offset:49152\n"
  "v_mfma_f32_16x16x32_bf16 v[56:59], a[0:3], v[44:47], 0\n"
  "s_lshl_b32 s98, s60, 24\n"
  "s_lshl_b32 s66, s60, 15\n"
  "v_mfma_f32_16x16x32_bf16 v[12:15], a[192:195], v[44:47], 0\n"
  "s_lshr_b32 s88, %[xstr], 1\n"
  "s_add_u32 s66, s66, s88\n"
  "v_mfma_f32_16x16x32_bf16 v[60:63], a[64:67], v[44:47], 0\n"
  "s_add_u32 s88, s66, 0x200000\n"
  "s_sub_u32 s66, s88, 0x8000\n"
  "v_mfma_f32_16x16x32_bf16 v[0:3], a[128:131], v[40:43], 0\n"
  "s_add_u32 s73, s66, 4096\n"
  "s_add_u32 s88, s88, %[rot]\n"
  "v_mfma_f32_16x16x32_bf16 v[48:51], a[0:3], v[40:43], 0\n"
  "s_xor_b32 s89, s88, 2048\n"
  "s_xor_b32 s30, s88, 4096\n"
  "v_mfma_f32_16x16x32_bf16 v[4:7], a[192:195], v[40:43], 0\n"
  "s_xor_b32 s31, s88, 6144\n"
  "s_mov_b32 s63, 0\n"
  "v_mfma_f32_16x16x32_bf16 v[52:55], a[64:67], v[40:43], 0\n"
  "s_lshl_b32 s71, s60, 18\n"
  "s_waitcnt lgkmcnt(2)\n"
  "v_mfma_f32_16x16x32_bf16 v[8:11], a[132:135], v[72:75], v[8:11]\n"
  "ds_read_b128 v[40:43], v111 offset:32768\n"
  "ds_read_b128 v[44:47], v111 offset:49152\n"
  "v_mfma_f32_16x16x32_bf16 v[56:59], a[4:7], v[72:75], v[56:59]\n"
  "s_add_u32 s71, s71, %[obase]\n"
  "s_add_u32 s72, s71, 0x20000\n"
  "v_mfma_f32_16x16x32_bf16 v[12:15], a[196:199], v[72:75], v[12:15]\n"
  "s_add_u32 s90, s60, 1\n"
  "s_mov_b32 s62, 1\n"
  "v_mfma_f32_16x16x32_bf16 v[60:63], a[68:71], v[72:75], v[60:63]\n"
  "s_waitcnt vmcnt(2)\n"
  "v_add_f32 v84, v84, v96\n"
  "v_mfma_f32_16x16x32_bf16 v[0:3], a[132:135], v[68:71], v[0:3]\n"
  "v_add_f32 v85, v85, v97\n"
  "v_mfma_f32_16x16x32_bf16 v[48:51], a[4:7], v[68:71], v[48:51]\n"
  "s_waitcnt vmcnt(1)\n"
  "v_add_f32 v86, v86, v98\n"
  "v_mfma_f32_16x16x32_bf16 v[4:7], a[196:199], v[68:71], v[4:7]\n"
  "v_add_f32 v87, v87, v99\n"
  "v_add_f32 v76, v76, v80\n"
  "v_mfma_f32_16x16x32_bf16 v[52:55], a[68:71], v[68:71], v[52:55]\n"
  "v_add_f32 v77, v77, v81\n"
  "s_waitcnt lgkmcnt(2)\n"
  "v_mfma_f32_16x16x32_bf16 v[8:11], a[136:139], v[36:39], v[8:11]\n"
  "ds_read_b128 v[68:71], v108 offset:33024\n"
  "ds_read_b128 v[72:75], v108 offset:49408\n"
  "v_mfma_f32_16x16x32_bf16 v[56:59], a[8:11], v[36:39], v[56:59]\n"
  "v_add_f32 v78, v78, v82\n"
  "v_add_f32 v79, v79, v83\n"
  "v_mfma_f32_16x16x32_bf16 v[12:15], a[200:203], v[36:39], v[12:15]\n"
  "v_add_f32 v76, v76, v84\n"
  "v_add_f32 v77, v77, v85\n"
  "v_mfma_f32_16x16x32_bf16 v[60:63], a[72:75], v[36:39], v[60:63]\n"
  "v_add_f32 v78, v78, v86\n"
  "v_add_f32 v79, v79, v87\n"
  "v_mfma_f32_16x16x32_bf16 v[0:3], a[136:139], v[32:35], v[0:3]\n"
  "v_add_f32 v64, v76, v78\n"
  "v_add_f32 v65, v77, v79\n"
  "v_mfma_f32_16x16x32_bf16 v[48:51], a[8:11], v[32:35], v[48:51]\n"
  "v_lshrrev_b32 v77, 5, v122\n"
  "s_add_u32 s68, %[wo], 31744\n"
  "v_mfma_f32_16x16x32_bf16 v[4:7], a[200:203], v[32:35], v[4:7]\n"
  "v_permlane32_swap_b32 v64, v65\n"
  "v_add_f32 v64, v64, v65\n"
  "v_mfma_f32_16x16x32_bf16 v[52:55], a[72:75], v[32:35], v[52:55]\n"
  "v_mov_b32 v65, v64\n"
  "s_waitcnt lgkmcnt(2)\n"
  "v_mfma_f32_16x16x32_bf16 v[8:11], a[140:143], v[44:47], v[8:11]\n"
  "ds_read_b128 v[32:35], v109 offset:33024\n"
  "ds_read_b128 v[36:39], v109 offset:49408\n"
  "v_mfma_f32_16x16x32_bf16 v[56:59], a[12:15], v[44:47], v[56:59]\n"
  "v_add_u32 v77, s68, v77\n"
  "v_mov_b32 v76, 0\n"
  "v_mfma_f32_16x16x32_bf16 v[12:15], a[204:207], v[44:47], v[12:15]\n"
  "v_permlane32_swap_b32 v64, v65\n"
  "v_mul_f32 v66, 0x3b000000, v64\n"
  "v_mfma_f32_16x16x32_bf16 v[60:63], a[76:79], v[44:47], v[60:63]\n"
  "v_mul_f32 v65, 0x3b000000, v65\n"
  "v_fma_f32 v65, -v66, v66, v65\n"
  "v_mfma_f32_16x16x32_bf16 v[0:3], a[140:143], v[40:43], v[0:3]\n"
  "v_max_f32 v65, 0, v65\n"
  "v_add_f32 v65, 0x3727c5ac, v65\n"
  "v_mfma_f32_16x16x32_bf16 v[48:51], a[12:15], v[40:43], v[48:51]\n"
  "v_rsq_f32 v64, v65\n"
  "v_mfma_f32_16x16x32_bf16 v[4:7], a[204:207], v[40:43], v[4:7]\n"
  "ds_write_b32 v123, v76\n"
  "v_mul_f32 v66, -v64, v66\n"
  "v_mfma_f32_16x16x32_bf16 v[52:55], a[76:79], v[40:43], v[52:55]\n"
  "v_mov_b32 v65, v64\n"
  "s_waitcnt lgkmcnt(3)\n"
  "v_mfma_f32_16x16x32_bf16 v[8:11], a[144:147], v[72:75], v[8:11]\n"
  "ds_read_b128 v[40:43], v110 offset:33024\n"
  "ds_read_b128 v[44:47], v110 offset:49408\n"
  "v_mfma_f32_16x16x32_bf16 v[56:59], a[16:19], v[72:75], v[56:59]\n"
  "v_mov_b32 v67, v66\n"
  "ds_write2_b32 v77, v64, v66 offset0:64 offset1:96\n"
  "v_mfma_f32_16x16x32_bf16 v[12:15], a[208:211], v[72:75], v[12:15]\n"
  "v_lshlrev_b32 v103, 8, v104\n"
  "v_and_b32 v103, 0x1000, v103\n"
  "v_mfma_f32_16x16x32_bf16 v[60:63], a[80:83], v[72:75], v[60:63]\n"
  "s_lshl_b32 s57, %[xstr], 5\n"
  "s_sub_u32 s57, 16384, s57\n"
  "v_mfma_f32_16x16x32_bf16 v[0:3], a[144:147], v[68:71], v[0:3]\n"
  "buffer_load_dword v102, v122, %[rws], %[huin] offen sc1\n"
  "s_nop 0\n"
  "v_mfma_f32_16x16x32_bf16 v[48:51], a[16:19], v[68:71], v[48:51]\n"
  "buffer_load_dword v103, v103, %[rws], s57 offen sc1\n"
  "v_permlane16_swap_b32 v64, v65\n"
  "v_mfma_f32_16x16x32_bf16 v[4:7], a[208:211], v[68:71], v[4:7]\n"
  "v_permlane16_swap_b32 v66, v67\n"
  "ds_read_b128 v[76:79], v118 offset:256\n"
  "v_mfma_f32_16x16x32_bf16 v[52:55], a[80:83], v[68:71], v[52:55]\n"
  "ds_read_b128 v[80:83], v118 offset:384\n"
  "s_waitcnt lgkmcnt(6)\n"
  "v_mfma_f32_16x16x32_bf16 v[8:11], a[148:151], v[36:39], v[8:11]\n"
  "ds_read_b128 v[68:71], v111 offset:33024\n"
  "ds_read_b128 v[72:75], v111 offset:49408\n"
  "v_mfma_f32_16x16x32_bf16 v[56:59], a[20:23], v[36:39], v[56:59]\n"
  "ds_read_b128 v[84:87], v118 offset:320\n"
  "ds_read_b128 v[96:99], v118 offset:448\n"
  "v_mfma_f32_16x16x32_bf16 v[12:15], a[212:215], v[36:39], v[12:15]\n"
  "s_waitcnt lgkmcnt(4)\n"
  "v_mul_f32 v100, v80, v120\n"
  "v_mfma_f32_16x16x32_bf16 v[60:63], a[84:87], v[36:39], v[60:63]\n"
  "v_mul_f32 v101, v81, v120\n"
  "v_fmac_f32 v100, v76, v24\n"
  "v_mfma_f32_16x16x32_bf16 v[0:3], a[148:151], v[32:35], v[0:3]\n"
  "v_fmac_f32 v101, v77, v25\n"
  "v_cvt_pk_bf16_f32 v88, v100, v101\n"
  "v_mfma_f32_16x16x32_bf16 v[48:51], a[20:23], v[32:35], v[48:51]\n"
  "v_mul_f32 v100, v82, v120\n"
  "v_mul_f32 v101, v83, v120\n"
  "v_mfma_f32_16x16x32_bf16 v[4:7], a[212:215], v[32:35], v[4:7]\n"
  "v_fmac_f32 v100, v78, v26\n"
  "v_fmac_f32 v101, v79, v27\n"
  "v_mfma_f32_16x16x32_bf16 v[52:55], a[84:87], v[32:35], v[52:55]\n"
  "v_cvt_pk_bf16_f32 v89, v100, v101\n"
  "v_mul_f32 v100, v80, v121\n"
  "v_mfma_f32_16x16x32_bf16 v[8:11], a[152:155], v[44:47], v[8:11]\n"
  "ds_read_b128 v[32:35], v108 offset:33280\n"
  "ds_read_b128 v[36:39], v108 offset:49664\n"
  "v_mfma_f32_16x16x32_bf16 v[56:59], a[24:27], v[44:47], v[56:59]\n"
  "v_mul_f32 v101, v81, v121\n"
  "v_fmac_f32 v100, v76, v28\n"
  "v_mfma_f32_16x16x32_bf16 v[12:15], a[216:219], v[44:47], v[12:15]\n"
  "v_fmac_f32 v101, v77, v29\n"
  "v_cvt_pk_bf16_f32 v92, v100, v101\n"
  "v_mfma_f32_16x16x32_bf16 v[60:63], a[88:91], v[44:47], v[60:63]\n"
  "v_mul_f32 v100, v82, v121\n"
  "v_mul_f32 v101, v83, v121\n"
  "v_mfma_f32_16x16x32_bf16 v[0:3], a[152:155], v[40:43], v[0:3]\n"
  "v_fmac_f32 v100, v78, v30\n"
  "v_fmac_f32 v101, v79, v31\n"
  "v_mfma_f32_16x16x32_bf16 v[48:51], a[24:27], v[40:43], v[48:51]\n"
  "v_cvt_pk_bf16_f32 v93, v100, v101\n"
  "ds_read_b128 v[76:79], v118 offset:128\n"
  "v_mfma_f32_16x16x32_bf16 v[4:7], a[216:219], v[40:43], v[4:7]\n"
  "ds_read_b128 v[80:83], v118 offset:192\n"
  "v_mfma_f32_16x16x32_bf16 v[52:55], a[88:91], v[40:43], v[52:55]\n"
  "s_waitcnt lgkmcnt(6)\n"
  "v_mfma_f32_16x16x32_bf16 v[8:11], a[156:159], v[72:75], v[8:11]\n"
  "ds_read_b128 v[40:43], v109 offset:33280\n"
  "ds_read_b128 v[44:47], v109 offset:49664\n"
  "v_mfma_f32_16x16x32_bf16 v[56:59], a[28:31], v[72:75], v[56:59]\n"
  "s_waitcnt lgkmcnt(6)\n"
  "v_mul_f32 v100, v96, v120\n"
  "v_mfma_f32_16x16x32_bf16 v[12:15], a[220:223], v[72:75], v[12:15]\n"
  "v_mul_f32 v101, v97, v120\n"
  "v_fmac_f32 v100, v84, v16\n"
  "v_mfma_f32_16x16x32_bf16 v[60:63], a[92:95], v[72:75], v[60:63]\n"
  "v_fmac_f32 v101, v85, v17\n"
  "v_cvt_pk_bf16_f32 v90, v100, v101\n"
  "v_mfma_f32_16x16x32_bf16 v[0:3], a[156:159], v[68:71], v[0:3]\n"
  "v_mul_f32 v100, v98, v120\n"
  "v_mul_f32 v101, v99, v120\n"
  "v_mfma_f32_16x16x32_bf16 v[48:51], a[28:31], v[68:71], v[48:51]\n"
  "v_fmac_f32 v100, v86, v18\n"
  "v_fmac_f32 v101, v87, v19\n"
  "v_mfma_f32_16x16x32_bf16 v[4:7], a[220:223], v[68:71], v[4:7]\n"
  "v_cvt_pk_bf16_f32 v91, v100, v101\n"
  "v_mul_f32 v100, v96, v121\n"
  "v_mfma_f32_16x16x32_bf16 v[52:55], a[92:95], v[68:71], v[52:55]\n"
  "v_mul_f32 v101, v97, v121\n"
  "s_waitcnt lgkmcnt(4)\n"
  "v_mfma_f32_16x16x32_bf16 v[8:11], a[160:163], v[36:39], v[8:11]\n"
  "ds_read_b128 v[68:71], v110 offset:33280\n"
  "ds_read_b128 v[72:75], v110 offset:49664\n"
  "v_mfma_f32_16x16x32_bf16 v[56:59], a[32:35], v[36:39], v[56:59]\n"
  "v_fmac_f32 v100, v84, v20\n"
  "v_fmac_f32 v101, v85, v21\n"
  "v_mfma_f32_16x16x32_bf16 v[12:15], a[224:227], v[36:39], v[12:15]\n"
  "v_cvt_pk_bf16_f32 v94, v100, v101\n"
  "v_mul_f32 v100, v98, v121\n"
  "v_mfma_f32_16x16x32_bf16 v[60:63], a[96:99], v[36:39], v[60:63]\n"
  "v_mul_f32 v101, v99, v121\n"
  "v_fmac_f32 v100, v86, v22\n"
  "v_mfma_f32_16x16x32_bf16 v[0:3], a[160:163], v[32:35], v[0:3]\n"
  "v_fmac_f32 v101, v87, v23\n"
  "v_cvt_pk_bf16_f32 v95, v100, v101\n"
  "v_mfma_f32_16x16x32_bf16 v[48:51], a[32:35], v[32:35], v[48:51]\n"
  "v_bfe_u32 v85, v104, 4, 4\n"
  "v_mul_u32_u24 v85, %[xstr], v85\n"
  "v_mfma_f32_16x16x32_bf16 v[4:7], a[224:227], v[32:35], v[4:7]\n"
  "v_lshrrev_b32 v84, 4, v104\n"
  "v_and_b32 v84, 8, v84\n"
  "v_mfma_f32_16x16x32_bf16 v[52:55], a[96:99], v[32:35], v[52:55]\n"
  "s_lshl_b32 s68, %[wave], 9\n"
  "s_waitcnt lgkmcnt(2)\n"
  "v_mfma_f32_16x16x32_bf16 v[8:11], a[164:167], v[44:47], v[8:11]\n"
  "ds_read_b128 v[32:35], v111 offset:33280\n"
  "ds_read_b128 v[36:39], v111 offset:49664\n"
  "v_mfma_f32_16x16x32_bf16 v[56:59], a[36:39], v[44:47], v[56:59]\n"
  "s_memrealtime s[100:101]\n"
  "v_add3_u32 v84, v84, v115, s68\n"
  "v_mfma_f32_16x16x32_bf16 v[12:15], a[228:231], v[44:47], v[12:15]\n"
  "v_mfma_f32_16x16x32_bf16 v[60:63], a[100:103], v[44:47], v[60:63]\n"
  "v_mfma_f32_16x16x32_bf16 v[0:3], a[164:167], v[40:43], v[0:3]\n"
  "v_mfma_f32_16x16x32_bf16 v[48:51], a[36:39], v[40:43], v[48:51]\n"
  "v_mfma_f32_16x16x32_bf16 v[4:7], a[228:231], v[40:43], v[4:7]\n"
  "v_mfma_f32_16x16x32_bf16 v[52:55], a[100:103], v[40:43], v[52:55]\n"
  "s_waitcnt lgkmcnt(0)\n"
  "v_mfma_f32_16x16x32_bf16 v[8:11], a[168:171], v[72:75], v[8:11]\n"
  "ds_read_b128 v[40:43], v108 offset:33536\n"
  "ds_read_b128 v[44:47], v108 offset:49920\n"
  "v_mfma_f32_16x16x32_bf16 v[56:59], a[40:43], v[72:75], v[56:59]\n"
  "s_waitcnt vmcnt(0)\n"
  "v_readlane_b32 s57, v103, 0\n"
  "v_mfma_f32_16x16x32_bf16 v[12:15], a[232:235], v[72:75], v[12:15]\n"
  "v_readlane_b32 s56, v103, 1\n"
  "s_nop 0\n"
  "v_mfma_f32_16x16x32_bf16 v[60:63], a[104:107], v[72:75], v[60:63]\n"
  "v_subrev_u32 v103, s100, v102\n"
  "v_bfe_i32 v103, v103, 0, 16\n"
  "v_mfma_f32_16x16x32_bf16 v[0:3], a[168:171], v[68:71], v[0:3]\n"
  "v_cmp_gt_u32 vcc, s98, v102\n"
  "v_readfirstlane_b32 s64, v102\n"
  "v_mfma_f32_16x16x32_bf16 v[48:51], a[40:43], v[68:71], v[48:51]\n"
  "v_max_i32_dpp v103, v103, v103 row_ror:8 row_mask:0xf bank_mask:0xf bound_ctrl:1\n"
  "s_cmp_eq_u64 vcc, 0\n"
  "v_mfma_f32_16x16x32_bf16 v[4:7], a[232:235], v[68:71], v[4:7]\n"
  "s_cselect_b32 s74, 1, 0\n"
  "v_max_i32_dpp v103, v103, v103 row_ror:4 row_mask:0xf bank_mask:0xf bound_ctrl:1\n"
  "v_mfma_f32_16x16x32_bf16 v[52:55], a[104:107], v[68:71], v[52:55]\n"
  "s_lshl_b32 s65, s64, 7\n"
  "s_and_b32 s65, s65, 0x7f800000\n"
  "v_mfma_f32_16x16x32_bf16 v[8:11], a[172:175], v[36:39], v[8:11]\n"
  "ds_read_b128 v[68:71], v109 offset:33536\n"
  "ds_read_b128 v[72:75], v109 offset:49920\n"
  "v_mfma_f32_16x16x32_bf16 v[56:59], a[44:47], v[36:39], v[56:59]\n"
  "v_max_i32_dpp v103, v103, v103 row_ror:2 row_mask:0xf bank_mask:0xf bound_ctrl:1\n"
  "s_add_u32 s75, s65, 0xd800000\n"
  "v_mfma_f32_16x16x32_bf16 v[12:15], a[236:239], v[36:39], v[12:15]\n"
  "s_cmp_lg_u32 s70, 0\n"
  "v_max_i32_dpp v103, v103, v103 row_ror:1 row_mask:0xf bank_mask:0xf bound_ctrl:1\n"
  "v_mfma_f32_16x16x32_bf16 v[60:63], a[108:111], v[36:39], v[60:63]\n"
  "s_cselect_b32 s74, s74, 0\n"
  "v_readlane_b32 s29, v103, 0\n"
  "v_mfma_f32_16x16x32_bf16 v[0:3], a[172:175], v[32:35], v[0:3]\n"
  "v_readlane_b32 s67, v103, 16\n"
  "s_max_i32 s29, s29, s67\n"
  "v_mfma_f32_16x16x32_bf16 v[48:51], a[44:47], v[32:35], v[48:51]\n"
  "s_cmp_lt_u32 s29, 0x200\n"
  "s_cselect_b32 s29, s29, 0\n"
  "v_mfma_f32_16x16x32_bf16 v[4:7], a[236:239], v[32:35], v[4:7]\n"
  "s_cmp_eq_u32 s74, 1\n"
  "s_cselect_b32 s29, s29, 0x10000000\n"
  "v_mfma_f32_16x16x32_bf16 v[52:55], a[108:111], v[32:35], v[52:55]\n"
  "s_add_u32 s29, s100, s29\n"
  "s_waitcnt lgkmcnt(2)\n"
  "v_mfma_f32_16x16x32_bf16 v[8:11], a[176:179], v[44:47], v[8:11]\n"
  "ds_read_b128 v[32:35], v110 offset:33536\n"
  "ds_read_b128 v[36:39], v110 offset:49920\n"
  "v_mfma_f32_16x16x32_bf16 v[56:59], a[48:51], v[44:47], v[56:59]\n"
  "ds_read_b128 v[96:99], v118 offset:0\n"
  "ds_read_b128 v[100:103], v118 offset:64\n"
  "v_mfma_f32_16x16x32_bf16 v[12:15], a[240:243], v[44:47], v[12:15]\n"
  "s_cmp_eq_u32 %[xstr], 0\n"
  "s_cselect_b32 s67, 47, 87\n"
  "v_mfma_f32_16x16x32_bf16 v[60:63], a[112:115], v[44:47], v[60:63]\n"
  "s_min_u32 s68, s57, s56\n"
  "s_cmp_lg_u32 s68, 0\n"
  "v_mfma_f32_16x16x32_bf16 v[0:3], a[176:179], v[40:43], v[0:3]\n"
  "s_cselect_b32 s67, s67, 0\n"
  "s_sub_u32 s57, s57, s29\n"
  "v_mfma_f32_16x16x32_bf16 v[48:51], a[48:51], v[40:43], v[48:51]\n"
  "s_add_u32 s57, s57, 72\n"
  "s_and_b32 s57, s57, 0xffff\n"
  "v_mfma_f32_16x16x32_bf16 v[4:7], a[240:243], v[40:43], v[4:7]\n"
  "s_cmp_lt_u32 s57, s67\n"
  "s_cselect_b32 s57, s57, 0\n"
  "v_mfma_f32_16x16x32_bf16 v[52:55], a[112:115], v[40:43], v[52:55]\n"
  "s_sub_u32 s56, s56, s29\n"
  "s_waitcnt lgkmcnt(4)\n"
  "v_mfma_f32_16x16x32_bf16 v[8:11], a[180:183], v[72:75], v[8:11]\n"
  "ds_read_b128 v[40:43], v111 offset:33536\n"
  "ds_read_b128 v[44:47], v111 offset:49920\n"
  "v_mfma_f32_16x16x32_bf16 v[56:59], a[52:55], v[72:75], v[56:59]\n"
  "s_memrealtime s[100:101]\n"
  "s_add_u32 s56, s56, 72\n"
  "v_mfma_f32_16x16x32_bf16 v[12:15], a[244:247], v[72:75], v[12:15]\n"
  "s_and_b32 s56, s56, 0xffff\n"
  "s_cmp_lt_u32 s56, s67\n"
  "v_mfma_f32_16x16x32_bf16 v[60:63], a[116:119], v[72:75], v[60:63]\n"
  "s_cselect_b32 s56, s56, 0\n"
  "s_max_u32 s68, s57, s56\n"
  "v_mfma_f32_16x16x32_bf16 v[0:3], a[180:183], v[68:71], v[0:3]\n"
  "s_add_u32 s29, s29, s68\n"
  "v_mfma_f32_16x16x32_bf16 v[48:51], a[52:55], v[68:71], v[48:51]\n"
  "v_mfma_f32_16x16x32_bf16 v[4:7], a[244:247], v[68:71], v[4:7]\n"
  "s_waitcnt lgkmcnt(0)\n"
  "s_sub_u32 s67, s29, s100\n"
  "v_mfma_f32_16x16x32_bf16 v[52:55], a[116:119], v[68:71], v[52:55]\n"
  "s_cmp_le_i32 s67, 4\n"
  "s_cbranch_scc1 .Lh_cp0_%=\n"
  ".Lh_cp0r_%=:\n"
  "v_mfma_f32_16x16x32_bf16 v[8:11], a[184:187], v[36:39], v[8:11]\n"
  "v_mfma_f32_16x16x32_bf16 v[56:59], a[56:59], v[36:39], v[56:59]\n"
  "s_memrealtime s[100:101]\n"
  "v_mfma_f32_16x16x32_bf16 v[12:15], a[248:251], v[36:39], v[12:15]\n"
  "v_mfma_f32_16x16x32_bf16 v[60:63], a[120:123], v[36:39], v[60:63]\n"
  "v_mfma_f32_16x16x32_bf16 v[0:3], a[184:187], v[32:35], v[0:3]\n"
  "v_mfma_f32_16x16x32_bf16 v[48:51], a[56:59], v[32:35], v[48:51]\n"
  "v_mfma_f32_16x16x32_bf16 v[4:7], a[248:251], v[32:35], v[4:7]\n"
  "s_waitcnt lgkmcnt(0)\n"
  "s_sub_u32 s67, s29, s100\n"
  "v_mfma_f32_16x16x32_bf16 v[52:55], a[120:123], v[32:35], v[52:55]\n"
  "s_cmp_le_i32 s67, 4\n"
  "s_cbranch_scc1 .Lh_cp1_%=\n"
  ".Lh_cp1r_%=:\n"
  "v_mfma_f32_16x16x32_bf16 v[8:11], a[188:191], v[44:47], v[8:11]\n"
  "v_mfma_f32_16x16x32_bf16 v[56:59], a[60:63], v[44:47], v[56:59]\n"
  "v_mfma_f32_16x16x32_bf16 v[12:15], a[252:255], v[44:47], v[12:15]\n"
  "v_mfma_f32_16x16x32_bf16 v[60:63], a[124:127], v[44:47], v[60:63]\n"
  "s_memrealtime s[100:101]\n"
  "v_mfma_f32_16x16x32_bf16 v[0:3], a[188:191], v[40:43], v[0:3]\n"
  "v_mfma_f32_16x16x32_bf16 v[48:51], a[60:63], v[40:43], v[48:51]\n"
  "v_mfma_f32_16x16x32_bf16 v[4:7], a[252:255], v[40:43], v[4:7]\n"
  "v_mfma_f32_16x16x32_bf16 v[52:55], a[124:127], v[40:43], v[52:55]\n"
  "s_barrier\n"
  "s_cmp_eq_u32 s63, 0\n"
  "s_cbranch_scc1 .Lh_wait_%=\n"
  ".Lh_gota_%=:\n"
  ".Lh_pre_%=:\n"
  "v_mov_b64 v[68:69], 0\n"
  "v_mov_b64 v[70:71], 0\n"
  "v_mov_b64 v[72:73], 0\n"
  "v_mov_b64 v[74:75], 0\n"
  "v_mov_b32 v86, 0xffff\n"
  "v_mov_b32 v87, 0xffff0000\n"
  "v_cndmask_b32_e64 v87, v87, v86, s[76:77]\n"
  "v_cndmask_b32_e64 v86, 0, v87, s[78:79]\n"
  "v_cndmask_b32_e64 v87, v87, 0, s[78:79]\n"
  "v_pk_mul_f32 v[40:41], v[76:77], v[66:67] op_sel:[0,0] op_sel_hi:[1,0]\n"
  "v_pk_mul_f32 v[42:43], v[78:79], v[66:67] op_sel:[0,0] op_sel_hi:[1,0]\n"
  "v_pk_fma_f32 v[40:41], v[0:1], v[64:65], v[40:41] op_sel:[0,0,0] op_sel_hi:[1,0,1]\n"
  "v_pk_fma_f32 v[42:43], v[2:3], v[64:65], v[42:43] op_sel:[0,0,0] op_sel_hi:[1,0,1]\n"
  "v_cvt_pk_bf16_f32 v0, v40, v41\n"
  "v_cvt_pk_bf16_f32 v1, v42, v43\n"
  "v_pk_mul_f32 v[40:41], v[80:81], v[66:67] op_sel:[0,0] op_sel_hi:[1,0]\n"
  "v_pk_mul_f32 v[42:43], v[82:83], v[66:67] op_sel:[0,0] op_sel_hi:[1,0]\n"
  "v_pk_fma_f32 v[40:41], v[4:5], v[64:65], v[40:41] op_sel:[0,0,0] op_sel_hi:[1,0,1]\n"
  "v_pk_fma_f32 v[42:43], v[6:7], v[64:65], v[42:43] op_sel:[0,0,0] op_sel_hi:[1,0,1]\n"
  "v_cvt_pk_bf16_f32 v2, v40, v41\n"
  "v_cvt_pk_bf16_f32 v3, v42, v43\n"
  "v_pk_mul_f32 v[40:41], v[76:77], v[66:67] op_sel:[0,1] op_sel_hi:[1,1]\n"
  "v_pk_mul_f32 v[42:43], v[78:79], v[66:67] op_sel:[0,1] op_sel_hi:[1,1]\n"
  "v_pk_fma_f32 v[40:41], v[8:9], v[64:65], v[40:41] op_sel:[0,1,0] op_sel_hi:[1,1,1]\n"
  "v_pk_fma_f32 v[42:43], v[10:11], v[64:65], v[42:43] op_sel:[0,1,0] op_sel_hi:[1,1,1]\n"
  "v_cvt_pk_bf16_f32 v8, v40, v41\n"
  "v_cvt_pk_bf16_f32 v9, v42, v43\n"
  "v_pk_mul_f32 v[40:41], v[80:81], v[66:67] op_sel:[0,1] op_sel_hi:[1,1]\n"
  "v_pk_mul_f32 v[42:43], v[82:83], v[66:67] op_sel:[0,1] op_sel_hi:[1,1]\n"
  "v_pk_fma_f32 v[40:41], v[12:13], v[64:65], v[40:41] op_sel:[0,1,0] op_sel_hi:[1,1,1]\n"
  "v_pk_fma_f32 v[42:43], v[14:15], v[64:65], v[42:43] op_sel:[0,1,0] op_sel_hi:[1,1,1]\n"
  "v_cvt_pk_bf16_f32 v10, v40, v41\n"
  "v_cvt_pk_bf16_f32 v11, v42, v43\n"
  "v_pk_mul_f32 v[40:41], v[96:97], v[66:67] op_sel:[0,0] op_sel_hi:[1,0]\n"
  "v_pk_mul_f32 v[42:43], v[98:99], v[66:67] op_sel:[0,0] op_sel_hi:[1,0]\n"
  "v_pk_fma_f32 v[40:41], v[48:49], v[64:65], v[40:41] op_sel:[0,0,0] op_sel_hi:[1,0,1]\n"
  "v_pk_fma_f32 v[42:43], v[50:51], v[64:65], v[42:43] op_sel:[0,0,0] op_sel_hi:[1,0,1]\n"
  "v_cvt_pk_bf16_f32 v48, v40, v41\n"
  "v_cvt_pk_bf16_f32 v49, v42, v43\n"
  "v_pk_mul_f32 v[40:41], v[100:101], v[66:67] op_sel:[0,0] op_sel_hi:[1,0]\n"
  "v_pk_mul_f32 v[42:43], v[102:103], v[66:67] op_sel:[0,0] op_sel_hi:[1,0]\n"
  "v_pk_fma_f32 v[40:41], v[52:53], v[64:65], v[40:41] op_sel:[0,0,0] op_sel_hi:[1,0,1]\n"
  "v_pk_fma_f32 v[42:43], v[54:55], v[64:65], v[42:43] op_sel:[0,0,0] op_sel_hi:[1,0,1]\n"
  "v_cvt_pk_bf16_f32 v50, v40, v41\n"
  "v_cvt_pk_bf16_f32 v51, v42, v43\n"
  "v_pk_mul_f32 v[40:41], v[96:97], v[66:67] op_sel:[0,1] op_sel_hi:[1,1]\n"
  "v_pk_mul_f32 v[42:43], v[98:99], v[66:67] op_sel:[0,1] op_sel_hi:[1,1]\n"
  "v_pk_fma_f32 v[40:41], v[56:57], v[64:65], v[40:41] op_sel:[0,1,0] op_sel_hi:[1,1,1]\n"
  "v_pk_fma_f32 v[42:43], v[58:59], v[64:65], v[42:43] op_sel:[0,1,0] op_sel_hi:[1,1,1]\n"
  "v_cvt_pk_bf16_f32 v56, v40, v41\n"
  "v_cvt_pk_bf16_f32 v57, v42, v43\n"
  "v_pk_mul_f32 v[40:41], v[100:101], v[66:67] op_sel:[0,1] op_sel_hi:[1,1]\n"
  "v_pk_mul_f32 v[42:43], v[102:103], v[66:67] op_sel:[0,1] op_sel_hi:[1,1]\n"
  "v_pk_fma_f32 v[40:41], v[60:61], v[64:65], v[40:41] op_sel:[0,1,0] op_sel_hi:[1,1,1]\n"
  "v_pk_fma_f32 v[42:43], v[62:63], v[64:65], v[42:43] op_sel:[0,1,0] op_sel_hi:[1,1,1]\n"
  "v_cvt_pk_bf16_f32 v58, v40, v41\n"
  "v_cvt_pk_bf16_f32 v59, v42, v43\n"
  "v_mov_b64 v[76:77], 0\n"
  "v_mov_b64 v[78:79], 0\n"
  "v_mov_b64 v[80:81], 0\n"
  "v_mov_b64 v[82:83], 0\n"
  "v_mov_b64 v[96:97], 0\n"
  "v_mov_b64 v[98:99], 0\n"
  "v_mov_b32 v64, 0x3f803f80\n"
  "v_mov_b32 v65, 0x3f803f80\n"
  "v_mov_b32 v66, 0x3f803f80\n"
  "v_mov_b32 v67, 0x3f803f80\n"
  "s_cmp_eq_u32 s60, 0\n"
  "s_cbranch_scc1 .Lh_zero_%=\n"
  "s_waitcnt vmcnt(2)\n"
  "v_cvt_f32_i32 v24, v24\n"
  "v_cvt_f32_i32 v25, v25\n"
  "v_cvt_f32_i32 v26, v26\n"
  "v_cvt_f32_i32 v27, v27\n"
  "v_cvt_f32_i32 v28, v28\n"
  "v_cvt_f32_i32 v29, v29\n"
  "v_cvt_f32_i32 v30, v30\n"
  "v_cvt_f32_i32 v31, v31\n"
  "v_pk_mul_f32 v[24:25], v[24:25], s[64:65] op_sel:[0,1] op_sel_hi:[1,1]\n"
  "v_pk_mul_f32 v[26:27], v[26:27], s[64:65] op_sel:[0,1] op_sel_hi:[1,1]\n"
  "v_pk_mul_f32 v[28:29], v[28:29], s[64:65] op_sel:[0,1] op_sel_hi:[1,1]\n"
  "v_pk_mul_f32 v[30:31], v[30:31], s[64:65] op_sel:[0,1] op_sel_hi:[1,1]\n"
  "v_cvt_pk_bf16_f32 v32, v24, v25\n"
  "v_cvt_pk_bf16_f32 v33, v26, v27\n"
  "v_cvt_pk_bf16_f32 v34, v28, v29\n"
  "v_cvt_pk_bf16_f32 v35, v30, v31\n"
  "ds_write_b128 v105, v[32:35] offset:32768\n"
  "s_waitcnt vmcnt(0)\n"
  "v_cvt_f32_i32 v16, v16\n"
  "v_cvt_f32_i32 v17, v17\n"
  "v_cvt_f32_i32 v18, v18\n"
  "v_cvt_f32_i32 v19, v19\n"
  "v_cvt_f32_i32 v20, v20\n"
  "v_cvt_f32_i32 v21, v21\n"
  "v_cvt_f32_i32 v22, v22\n"
  "v_cvt_f32_i32 v23, v23\n"
  "v_pk_mul_f32 v[16:17], v[16:17], s[64:65] op_sel:[0,1] op_sel_hi:[1,1]\n"
  "v_pk_mul_f32 v[18:19], v[18:19], s[64:65] op_sel:[0,1] op_sel_hi:[1,1]\n"
  "v_pk_mul_f32 v[20:21], v[20:21], s[64:65] op_sel:[0,1] op_sel_hi:[1,1]\n"
  "v_pk_mul_f32 v[22:23], v[22:23], s[64:65] op_sel:[0,1] op_sel_hi:[1,1]\n"
  "v_cvt_pk_bf16_f32 v36, v16, v17\n"
  "v_cvt_pk_bf16_f32 v37, v18, v19\n"
  "v_cvt_pk_bf16_f32 v38, v20, v21\n"
  "v_cvt_pk_bf16_f32 v39, v22, v23\n"
  "ds_write_b128 v105, v[36:39] offset:36864\n"
  "v_max3_f32 v4, |v16|, |v17|, |v18|\n"
  "v_max3_f32 v5, |v19|, |v20|, |v21|\n"
  "v_max3_f32 v6, |v22|, |v23|, |v24|\n"
  "v_max3_f32 v7, |v25|, |v26|, |v27|\n"
  "v_max3_f32 v4, v4, |v28|, |v29|\n"
  "v_max3_f32 v5, v5, |v30|, |v31|\n"
  "v_max3_f32 v4, v4, v5, v6\n"
  "v_max_f32 v4, v4, v7\n"
  "v_cmp_le_f32 vcc, s75, v4\n"
  "s_cbranch_vccnz .Lh_retry_%=\n"
  ".Lh_decd_%=:\n"
  "v_cmp_lt_f32 vcc, 1.0, v4\n"
  "s_cbranch_vccnz .Lh_max_%=\n"
  ".Lh_max_r_%=:\n"
  "s_waitcnt lgkmcnt(0)\n"
  "s_barrier\n"
  "ds_read_b128 v[16:19], v104 offset:32768\n"
  "ds_read_b128 v[20:23], v104 offset:33792\n"
  "ds_read_b32 v12, v123\n"
  "ds_read_b128 v[28:31], v104 offset:35840\n"
  "ds_read_b128 v[24:27], v104 offset:34816\n"
  "s_memrealtime s[100:101]\n"
  "ds_read_b128 v[36:39], v104 offset:37888\n"
  "ds_read_b128 v[32:35], v104 offset:36864\n"
  "ds_read_b128 v[44:47], v104 offset:39936\n"
  "ds_read_b128 v[40:43], v104 offset:38912\n"
  "s_add_u32 s68, s98, 0xea0000\n"
  "v_mov_b64 v[100:101], 0\n"
  "v_mov_b64 v[102:103], 0\n"
  "s_waitcnt lgkmcnt(8)\n"
  "v_smfmac_f32_16x16x64_bf16 v[68:71], v[16:19], a[0:7], v107\n"
  "v_smfmac_f32_16x16x64_bf16 v[72:75], v[16:19], a[64:71], v107\n"
  "s_waitcnt lgkmcnt(7)\n"
  "v_smfmac_f32_16x16x64_bf16 v[68:71], v[20:23], a[16:23], v107\n"
  "v_smfmac_f32_16x16x64_bf16 v[72:75], v[20:23], a[80:87], v107\n"
  "s_waitcnt lgkmcnt(4)\n"
  "v_smfmac_f32_16x16x64_bf16 v[68:71], v[24:27], a[32:39], v107\n"
  "v_max_f32 v12, 1.0, v12\n"
  "v_smfmac_f32_16x16x64_bf16 v[72:75], v[24:27], a[96:103], v107\n"
  "v_mul_f32 v12, s70, v12\n"
  "v_smfmac_f32_16x16x64_bf16 v[68:71], v[28:31], a[48:55], v107\n"
  "v_lshrrev_b32 v12, 23, v12\n"
  "v_smfmac_f32_16x16x64_bf16 v[72:75], v[28:31], a[112:119], v107\n"
  "s_waitcnt lgkmcnt(2)\n"
  "v_smfmac_f32_16x16x64_bf16 v[68:71], v[32:35], a[8:15], v107\n"
  "v_add_u32 v12, 1, v12\n"
  "v_smfmac_f32_16x16x64_bf16 v[72:75], v[32:35], a[72:79], v107\n"
  "v_max_u32 v12, 40, v12\n"
  "v_smfmac_f32_16x16x64_bf16 v[68:71], v[36:39], a[24:31], v107\n"
  "v_min_u32 v12, 0xdc, v12\n"
  "v_smfmac_f32_16x16x64_bf16 v[72:75], v[36:39], a[88:95], v107\n"
  "s_waitcnt lgkmcnt(0)\n"
  "v_smfmac_f32_16x16x64_bf16 v[68:71], v[40:43], a[40:47], v107\n"
  "s_waitcnt lgkmcnt(0)\n"
  "v_smfmac_f32_16x16x64_bf16 v[72:75], v[40:43], a[104:111], v107\n"
  "s_add_u32 s67, s100, s27\n"
  "v_smfmac_f32_16x16x64_bf16 v[68:71], v[44:47], a[56:63], v107\n"
  "s_and_b32 s67, s67, 0xffff\n"
  "v_smfmac_f32_16x16x64_bf16 v[72:75], v[44:47], a[120:127], v107\n"
  "s_add_u32 s67, s67, s68\n"
  "v_smfmac_f32_16x16x64_bf16 v[76:79], v[16:19], a[128:135], v107\n"
  "v_lshl_add_u32 v13, v12, 16, s67\n"
  "v_smfmac_f32_16x16x64_bf16 v[80:83], v[16:19], a[192:199], v107\n"
  "buffer_store_dword v13, off, %[rws], %[huout] sc1\n"
  "v_smfmac_f32_16x16x64_bf16 v[76:79], v[20:23], a[144:151], v107\n"
  "v_readfirstlane_b32 s99, v12\n"
  "v_smfmac_f32_16x16x64_bf16 v[80:83], v[20:23], a[208:215], v107\n"
  "v_permlane32_swap_b32 v68, v72\n"
  "v_smfmac_f32_16x16x64_bf16 v[76:79], v[24:27], a[160:167], v107\n"
  "v_permlane32_swap_b32 v69, v73\n"
  "v_smfmac_f32_16x16x64_bf16 v[80:83], v[24:27], a[224:231], v107\n"
  "v_permlane32_swap_b32 v70, v74\n"
  "v_smfmac_f32_16x16x64_bf16 v[76:79], v[28:31], a[176:183], v107\n"
  "v_permlane32_swap_b32 v71, v75\n"
  "v_smfmac_f32_16x16x64_bf16 v[80:83], v[28:31], a[240:247], v107\n"
  "v_add_f32 v68, v68, v72\n"
  "v_smfmac_f32_16x16x64_bf16 v[76:79], v[32:35], a[136:143], v107\n"
  "v_add_f32 v69, v69, v73\n"
  "v_smfmac_f32_16x16x64_bf16 v[80:83], v[32:35], a[200:207], v107\n"
  "v_add_f32 v70, v70, v74\n"
  "v_smfmac_f32_16x16x64_bf16 v[76:79], v[36:39], a[152:159], v107\n"
  "v_add_f32 v71, v71, v75\n"
  "v_smfmac_f32_16x16x64_bf16 v[80:83], v[36:39], a[216:223], v107\n"
  "v_cvt_pk_bf16_f32 v68, v68, v69\n"
  "v_smfmac_f32_16x16x64_bf16 v[76:79], v[40:43], a[168:175], v107\n"
  "v_cvt_pk_bf16_f32 v69, v70, v71\n"
  "v_smfmac_f32_16x16x64_bf16 v[80:83], v[40:43], a[232:239], v107\n"
  "ds_write_b64 v112, v[68:69] offset:53248\n"
  "v_smfmac_f32_16x16x64_bf16 v[76:79], v[44:47], a[184:191], v107\n"
  "ds_read_b64_tr_b16 v[52:53], v113 offset:53248\n"
  "v_smfmac_f32_16x16x64_bf16 v[80:83], v[44:47], a[248:255], v107\n"
  "ds_read_b64_tr_b16 v[54:55], v113 offset:53312\n"
  "v_smfmac_f32_16x16x64_bf16 v[96:99], v[16:19], v[128:135], v107\n"
  "s_mov_b32 s26, s100\n"
  "v_smfmac_f32_16x16x64_bf16 v[100:103], v[16:19], v[192:199], v107\n"
  "s_lshl_b32 s99, s99, 23\n"
  "s_nop 1\n"
  "v_permlane32_swap_b32 v76, v80\n"
  "v_permlane32_swap_b32 v77, v81\n"
  "v_permlane32_swap_b32 v78, v82\n"
  "v_permlane32_swap_b32 v79, v83\n"
  "v_add_f32 v76, v76, v80\n"
  "v_add_f32 v77, v77, v81\n"
  "v_add_f32 v78, v78, v82\n"
  "v_add_f32 v79, v79, v83\n"
  "v_cvt_pk_bf16_f32 v76, v76, v77\n"
  "v_cvt_pk_bf16_f32 v77, v78, v79\n"
  "ds_write_b64 v112, v[76:77] offset:53760\n"
  "ds_read_b64_tr_b16 v[60:61], v113 offset:53760\n"
  "ds_read_b64_tr_b16 v[62:63], v113 offset:53824\n"
  "s_waitcnt lgkmcnt(3)\n"
  "v_mfma_f32_16x16x32_bf16 v[68:71], v[0:3], v[52:55], 0\n"
  "v_mfma_f32_16x16x32_bf16 v[72:75], v[8:11], v[52:55], 0\n"
  "v_smfmac_f32_16x16x64_bf16 v[96:99], v[20:23], v[144:151], v107\n"
  "v_smfmac_f32_16x16x64_bf16 v[100:103], v[20:23], v[208:215], v107\n"
  "v_cndmask_b32_e64 v68, v72, v68, s[76:77]\n"
  "v_cndmask_b32_e64 v69, v73, v69, s[76:77]\n"
  "v_cndmask_b32_e64 v70, v74, v70, s[76:77]\n"
  "v_cndmask_b32_e64 v71, v75, v71, s[76:77]\n"
  "s_waitcnt lgkmcnt(0)\n"
  "v_mfma_f32_16x16x32_bf16 v[76:79], v[60:63], v[52:55], 0\n"
  "ds_write_b128 v105, v[68:71] offset:40960\n"
  "v_smfmac_f32_16x16x64_bf16 v[96:99], v[24:27], v[160:167], v107\n"
  "v_smfmac_f32_16x16x64_bf16 v[100:103], v[24:27], v[224:231], v107\n"
  "v_cndmask_b32_e64 v76, v77, v76, s[76:77]\n"
  "v_cndmask_b32_e64 v78, v79, v78, s[76:77]\n"
  "v_cndmask_b32_e64 v76, v78, v76, s[78:79]\n"
  "ds_write_b32 v116, v76 offset:45056\n"
  "v_smfmac_f32_16x16x64_bf16 v[96:99], v[28:31], v[176:183], v107\n"
  "v_smfmac_f32_16x16x64_bf16 v[100:103], v[28:31], v[240:247], v107\n"
  "v_smfmac_f32_16x16x64_bf16 v[96:99], v[32:35], v[136:143], v107\n"
  "v_smfmac_f32_16x16x64_bf16 v[100:103], v[32:35], v[200:207], v107\n"
  "s_waitcnt lgkmcnt(0)\n"
  "s_barrier\n"
  "ds_read_b128 v[68:71], v104 offset:40960\n"
  "ds_read_b128 v[72:75], v104 offset:41984\n"
  "ds_read_b128 v[76:79], v104 offset:43008\n"
  "ds_read_b128 v[80:83], v104 offset:44032\n"
  "ds_read_b128 v[4:7], v104 offset:45056\n"
  "v_smfmac_f32_16x16x64_bf16 v[96:99], v[36:39], v[152:159], v107\n"
  "v_smfmac_f32_16x16x64_bf16 v[100:103], v[36:39], v[216:223], v107\n"
  "v_smfmac_f32_16x16x64_bf16 v[96:99], v[40:43], v[168:175], v107\n"
  "v_smfmac_f32_16x16x64_bf16 v[100:103], v[40:43], v[232:239], v107\n"
  "v_smfmac_f32_16x16x64_bf16 v[96:99], v[44:47], v[184:191], v107\n"
  "v_smfmac_f32_16x16x64_bf16 v[100:103], v[44:47], v[248:255], v107\n"
  "s_waitcnt lgkmcnt(3)\n"
  "v_pk_add_f32 v[68:69], v[68:69], v[72:73]\n"
  "v_pk_add_f32 v[70:71], v[70:71], v[74:75]\n"
  "s_waitcnt lgkmcnt(1)\n"
  "v_pk_add_f32 v[76:77], v[76:77], v[80:81]\n"
  "v_pk_add_f32 v[78:79], v[78:79], v[82:83]\n"
  "s_waitcnt lgkmcnt(0)\n"
  "v_pk_add_f32 v[4:5], v[4:5], v[6:7]\n"
  "v_pk_add_f32 v[68:69], v[68:69], v[76:77]\n"
  "v_pk_add_f32 v[70:71], v[70:71], v[78:79]\n"
  "v_add_f32 v4, v4, v5\n"
  "v_max3_f32 v72, v68, v69, v70\n"
  "v_max3_f32 v72, v72, v71, v4\n"
  "v_permlane32_swap_b32 v96, v100\n"
  "v_permlane32_swap_b32 v97, v101\n"
  "v_max_f32_dpp v72, v72, v72 row_ror:8 row_mask:0xf bank_mask:0xf bound_ctrl:1\n"
  "v_mov_b32 v73, v72\n"
  "v_permlane32_swap_b32 v98, v102\n"
  "v_permlane32_swap_b32 v99, v103\n"
  "v_permlane16_swap_b32 v72, v73\n"
  "v_max_f32 v72, v72, v73\n"
  "v_mov_b32 v73, v72\n"
  "v_add_f32 v96, v96, v100\n"
  "v_add_f32 v97, v97, v101\n"
  "v_permlane32_swap_b32 v72, v73\n"
  "v_max_f32 v72, v72, v73\n"
  "v_mul_f32 v72, 0xbe0293ee, v72\n"
  "v_pk_fma_f32 v[68:69], v[68:69], s[94:95], v[72:73] op_sel_hi:[1,1,0]\n"
  "v_pk_fma_f32 v[70:71], v[70:71], s[94:95], v[72:73] op_sel_hi:[1,1,0]\n"
  "v_fma_f32 v4, v4, s94, v72\n"
  "v_exp_f32 v68, v68\n"
  "v_exp_f32 v69, v69\n"
  "v_exp_f32 v70, v70\n"
  "v_exp_f32 v71, v71\n"
  "v_cvt_pk_bf16_f32 v68, v68, v69\n"
  "v_exp_f32 v4, v4\n"
  "v_cvt_pk_bf16_f32 v69, v70, v71\n"
  "v_cndmask_b32_e64 v52, 0, v68, s[76:77]\n"
  "v_cndmask_b32_e64 v54, v68, 0, s[76:77]\n"
  "v_cndmask_b32_e64 v53, 0, v69, s[76:77]\n"
  "v_cndmask_b32_e64 v55, v69, 0, s[76:77]\n"
  "v_cvt_pk_bf16_f32 v4, v4, v4\n"
  "v_add_f32 v98, v98, v102\n"
  "v_mfma_f32_16x16x32_bf16 v[80:83], v[64:67], v[52:55], 0\n"
  "v_and_b32 v86, v86, v4\n"
  "v_and_b32 v87, v87, v4\n"
  "v_mfma_f32_16x16x32_bf16 v[72:75], v[88:91], v[52:55], 0\n"
  "v_add_f32 v99, v99, v103\n"
  "v_cvt_pk_bf16_f32 v96, v96, v97\n"
  "v_mfma_f32_16x16x16_bf16 v[80:83], v[64:65], v[86:87], v[80:83]\n"
  "v_cvt_pk_bf16_f32 v97, v98, v99\n"
  "v_mov_b32 v98, v96\n"
  "v_mov_b32 v99, v97\n"
  "v_mfma_f32_16x16x32_bf16 v[76:79], v[92:95], v[52:55], 0\n"
  "v_permlane32_swap_b32 v96, v98\n"
  "v_permlane32_swap_b32 v97, v99\n"
  "ds_read_b128 v[20:23], v117 offset:1024\n"
  "v_add_f32_dpp v80, v80, v80 row_ror:8 row_mask:0xf bank_mask:0xf bound_ctrl:1\n"
  "v_mfma_f32_16x16x16_bf16 v[72:75], v[96:97], v[86:87], v[72:75]\n"
  "v_rcp_f32 v80, v80\n"
  "v_mfma_f32_16x16x16_bf16 v[76:79], v[98:99], v[86:87], v[76:79]\n"
  "ds_read_b128 v[16:19], v117 offset:0\n"
  "ds_read_b128 v[28:31], v117 offset:9216\n"
  "ds_read_b128 v[24:27], v117 offset:8192\n"
  "s_sub_u32 s67, 0x8a000000, s99\n"
  "s_sub_u32 s68, 0x8b000000, s99\n"
  "v_add_f32_dpp v68, v72, v72 row_ror:8 row_mask:0xf bank_mask:0x3\n"
  "v_add_f32_dpp v69, v73, v73 row_ror:8 row_mask:0xf bank_mask:0x3\n"
  "v_add_f32_dpp v70, v74, v74 row_ror:8 row_mask:0xf bank_mask:0x3\n"
  "v_add_f32_dpp v71, v75, v75 row_ror:8 row_mask:0xf bank_mask:0x3\n"
  "v_add_f32_dpp v68, v76, v76 row_ror:8 row_mask:0xf bank_mask:0xc\n"
  "v_add_f32_dpp v69, v77, v77 row_ror:8 row_mask:0xf bank_mask:0xc\n"
  "v_add_f32_dpp v70, v78, v78 row_ror:8 row_mask:0xf bank_mask:0xc\n"
  "v_add_f32_dpp v71, v79, v79 row_ror:8 row_mask:0xf bank_mask:0xc\n"
  "v_pk_mul_f32 v[68:69], v[68:69], v[80:81] op_sel_hi:[1,0]\n"
  "v_pk_mul_f32 v[70:71], v[70:71], v[80:81] op_sel_hi:[1,0]\n"
  "v_cvt_pk_bf16_f32 v68, v68, v69\n"
  "v_cvt_pk_bf16_f32 v69, v70, v71\n"
  "ds_write_b64 v84, v[68:69] offset:49152\n"
  "ds_read_b128 v[36:39], v117 offset:17408\n"
  "ds_read_b128 v[32:35], v117 offset:16384\n"
  "ds_read_b128 v[44:47], v117 offset:25600\n"
  "ds_read_b128 v[40:43], v117 offset:24576\n"
  "v_mov_b32 v86, s67\n"
  "v_mov_b32 v87, s68\n"
  "s_waitcnt lgkmcnt(4)\n"
  "s_barrier\n"
  "ds_read_b128 v[68:71], v115 offset:49152\n"
  "ds_read_b128 v[72:75], v115 offset:49664\n"
  "ds_read_b128 v[76:79], v115 offset:50176\n"
  "ds_read_b128 v[80:83], v115 offset:50688\n"
  "buffer_load_dword v85, v85, %[rws], %[cntx] offen sc1\n"
  "s_waitcnt lgkmcnt(3)\n"
  "v_mfma_f32_16x16x32_bf16 v[4:7], v[16:19], v[68:71], 0\n"
  "v_mfma_f32_16x16x32_bf16 v[12:15], v[20:23], v[68:71], 0\n"
  "ds_read_b128 v[20:23], v117 offset:3072\n"
  "s_waitcnt lgkmcnt(3)\n"
  "v_mfma_f32_16x16x32_bf16 v[4:7], v[24:27], v[72:75], v[4:7]\n"
  "ds_read_b128 v[16:19], v117 offset:2048\n"
  "v_mfma_f32_16x16x32_bf16 v[12:15], v[28:31], v[72:75], v[12:15]\n"
  "ds_read_b128 v[28:31], v117 offset:11264\n"
  "s_waitcnt lgkmcnt(4)\n"
  "v_mfma_f32_16x16x32_bf16 v[4:7], v[32:35], v[76:79], v[4:7]\n"
  "ds_read_b128 v[24:27], v117 offset:10240\n"
  "v_mfma_f32_16x16x32_bf16 v[12:15], v[36:39], v[76:79], v[12:15]\n"
  "ds_read_b128 v[36:39], v117 offset:19456\n"
  "s_waitcnt lgkmcnt(5)\n"
  "v_mfma_f32_16x16x32_bf16 v[4:7], v[40:43], v[80:83], v[4:7]\n"
  "ds_read_b128 v[32:35], v117 offset:18432\n"
  "v_mfma_f32_16x16x32_bf16 v[12:15], v[44:47], v[80:83], v[12:15]\n"
  "ds_read_b128 v[44:47], v117 offset:27648\n"
  "s_waitcnt lgkmcnt(5)\n"
  "v_mfma_f32_16x16x32_bf16 v[52:55], v[16:19], v[68:71], 0\n"
  "ds_read_b128 v[40:43], v117 offset:26624\n"
  "v_mfma_f32_16x16x32_bf16 v[64:67], v[20:23], v[68:71], 0\n"
  "ds_read_b128 v[20:23], v117 offset:5120\n"
  "v_cndmask_b32_e64 v100, v12, v4, s[76:77]\n"
  "s_waitcnt lgkmcnt(5)\n"
  "v_mfma_f32_16x16x32_bf16 v[52:55], v[24:27], v[72:75], v[52:55]\n"
  "ds_read_b128 v[16:19], v117 offset:4096\n"
  "v_cndmask_b32_e64 v101, v13, v5, s[76:77]\n"
  "v_fmaak_f32 v100, v100, v86, 0x4b400000\n"
  "v_mfma_f32_16x16x32_bf16 v[64:67], v[28:31], v[72:75], v[64:67]\n"
  "ds_read_b128 v[28:31], v117 offset:13312\n"
  "v_fmaak_f32 v101, v101, v87, 0x4c400000\n"
  "s_waitcnt lgkmcnt(5)\n"
  "v_mfma_f32_16x16x32_bf16 v[52:55], v[32:35], v[76:79], v[52:55]\n"
  "ds_read_b128 v[24:27], v117 offset:12288\n"
  "buffer_atomic_add_x2 v[100:101], v119, %[rws], s88 offen offset:0\n"
  "v_cndmask_b32_e64 v102, v14, v6, s[76:77]\n"
  "v_mfma_f32_16x16x32_bf16 v[64:67], v[36:39], v[76:79], v[64:67]\n"
  "ds_read_b128 v[36:39], v117 offset:21504\n"
  "v_cndmask_b32_e64 v103, v15, v7, s[76:77]\n"
  "s_waitcnt lgkmcnt(5)\n"
  "v_mfma_f32_16x16x32_bf16 v[52:55], v[40:43], v[80:83], v[52:55]\n"
  "ds_read_b128 v[32:35], v117 offset:20480\n"
  "v_fmaak_f32 v102, v102, v86, 0x4b400000\n"
  "v_fmaak_f32 v103, v103, v87, 0x4c400000\n"
  "v_mfma_f32_16x16x32_bf16 v[64:67], v[44:47], v[80:83], v[64:67]\n"
  "ds_read_b128 v[44:47], v117 offset:29696\n"
  "buffer_atomic_add_x2 v[102:103], v119, %[rws], s88 offen offset:1024\n"
  "s_waitcnt lgkmcnt(5)\n"
  "v_mfma_f32_16x16x32_bf16 v[4:7], v[16:19], v[68:71], 0\n"
  "ds_read_b128 v[40:43], v117 offset:28672\n"
  "v_mfma_f32_16x16x32_bf16 v[12:15], v[20:23], v[68:71], 0\n"
  "ds_read_b128 v[20:23], v117 offset:7168\n"
  "v_cndmask_b32_e64 v100, v64, v52, s[76:77]\n"
  "s_waitcnt lgkmcnt(5)\n"
  "v_mfma_f32_16x16x32_bf16 v[4:7], v[24:27], v[72:75], v[4:7]\n"
  "ds_read_b128 v[16:19], v117 offset:6144\n"
  "v_cndmask_b32_e64 v101, v65, v53, s[76:77]\n"
  "v_fmaak_f32 v100, v100, v86, 0x4b400000\n"
  "v_mfma_f32_16x16x32_bf16 v[12:15], v[28:31], v[72:75], v[12:15]\n"
  "ds_read_b128 v[28:31], v117 offset:15360\n"
  "v_fmaak_f32 v101, v101, v87, 0x4c400000\n"
  "s_waitcnt lgkmcnt(5)\n"
  "v_mfma_f32_16x16x32_bf16 v[4:7], v[32:35], v[76:79], v[4:7]\n"
  "ds_read_b128 v[24:27], v117 offset:14336\n"
  "buffer_atomic_add_x2 v[100:101], v119, %[rws], s89 offen offset:0\n"
  "v_cndmask_b32_e64 v102, v66, v54, s[76:77]\n"
  "v_mfma_f32_16x16x32_bf16 v[12:15], v[36:39], v[76:79], v[12:15]\n"
  "ds_read_b128 v[36:39], v117 offset:23552\n"
  "v_cndmask_b32_e64 v103, v67, v55, s[76:77]\n"
  "s_waitcnt lgkmcnt(5)\n"
  "v_mfma_f32_16x16x32_bf16 v[4:7], v[40:43], v[80:83], v[4:7]\n"
  "ds_read_b128 v[32:35], v117 offset:22528\n"
  "v_fmaak_f32 v102, v102, v86, 0x4b400000\n"
  "v_fmaak_f32 v103, v103, v87, 0x4c400000\n"
  "v_mfma_f32_16x16x32_bf16 v[12:15], v[44:47], v[80:83], v[12:15]\n"
  "buffer_atomic_add_x2 v[102:103], v119, %[rws], s89 offen offset:1024\n"
  "s_waitcnt lgkmcnt(4)\n"
  "v_mfma_f32_16x16x32_bf16 v[52:55], v[16:19], v[68:71], 0\n"
  "ds_read_b128 v[40:43], v117 offset:30720\n"
  "v_mfma_f32_16x16x32_bf16 v[64:67], v[20:23], v[68:71], 0\n"
  "v_cndmask_b32_e64 v100, v12, v4, s[76:77]\n"
  "v_cndmask_b32_e64 v101, v13, v5, s[76:77]\n"
  "s_waitcnt lgkmcnt(3)\n"
  "v_mfma_f32_16x16x32_bf16 v[52:55], v[24:27], v[72:75], v[52:55]\n"
  "v_fmaak_f32 v100, v100, v86, 0x4b400000\n"
  "v_fmaak_f32 v101, v101, v87, 0x4c400000\n"
  "buffer_atomic_add_x2 v[100:101], v119, %[rws], s30 offen offset:0\n"
  "v_mfma_f32_16x16x32_bf16 v[64:67], v[28:31], v[72:75], v[64:67]\n"
  "v_cndmask_b32_e64 v102, v14, v6, s[76:77]\n"
  "v_cndmask_b32_e64 v103, v15, v7, s[76:77]\n"
  "s_waitcnt lgkmcnt(1)\n"
  "v_mfma_f32_16x16x32_bf16 v[52:55], v[32:35], v[76:79], v[52:55]\n"
  "v_fmaak_f32 v102, v102, v86, 0x4b400000\n"
  "v_fmaak_f32 v103, v103, v87, 0x4c400000\n"
  "buffer_atomic_add_x2 v[102:103], v119, %[rws], s30 offen offset:1024\n"
  "v_mfma_f32_16x16x32_bf16 v[64:67], v[36:39], v[76:79], v[64:67]\n"
  "s_waitcnt lgkmcnt(0)\n"
  "v_mfma_f32_16x16x32_bf16 v[52:55], v[40:43], v[80:83], v[52:55]\n"
  "v_mfma_f32_16x16x32_bf16 v[64:67], v[124:127], v[80:83], v[64:67]\n"
  "v_mfma_f32_16x16x32_bf16 v[72:75], v[0:3], v[48:51], 0\n"
  "v_mfma_f32_16x16x32_bf16 v[76:79], v[0:3], v[56:59], 0\n"
  "v_cndmask_b32_e64 v100, v64, v52, s[76:77]\n"
  "v_cndmask_b32_e64 v101, v65, v53, s[76:77]\n"
  "v_fmaak_f32 v100, v100, v86, 0x4b400000\n"
  "v_mfma_f32_16x16x32_bf16 v[68:71], v[60:63], v[48:51], 0\n"
  "v_fmaak_f32 v101, v101, v87, 0x4c400000\n"
  "buffer_atomic_add_x2 v[100:101], v119, %[rws], s31 offen offset:0\n"
  "v_cndmask_b32_e64 v102, v66, v54, s[76:77]\n"
  "v_mfma_f32_16x16x32_bf16 v[16:19], v[60:63], v[56:59], 0\n"
  "v_cndmask_b32_e64 v103, v67, v55, s[76:77]\n"
  "v_fmaak_f32 v102, v102, v86, 0x4b400000\n"
  "v_fmaak_f32 v103, v103, v87, 0x4c400000\n"
  "v_mfma_f32_16x16x32_bf16 v[80:83], v[8:11], v[56:59], 0\n"
  "buffer_atomic_add_x2 v[102:103], v119, %[rws], s31 offen offset:1024\n"
  "s_memrealtime s[100:101]\n"
  "ds_write_b128 v105, v[72:75] offset:36864\n"
  "v_xor_b32 v24, 0x400, v105\n"
  "v_xor_b32 v25, 0x800, v105\n"
  "v_xor_b32 v26, 0xc00, v105\n"
  "ds_write_b128 v105, v[76:79] offset:40960\n"
  "v_cndmask_b32_e64 v68, v16, v68, s[78:79]\n"
  "v_cndmask_b32_e64 v69, v17, v69, s[78:79]\n"
  "v_cndmask_b32_e64 v70, v18, v70, s[78:79]\n"
  "v_cndmask_b32_e64 v71, v19, v71, s[78:79]\n"
  "ds_write_b128 v105, v[80:83] offset:45056\n"
  "v_mov_b32 v27, 0xd028965f\n"
  "v_mov_b32 v28, 0xff800000\n"
  "v_xor_b32 v57, 16, v106\n"
  "v_xor_b32 v58, 32, v106\n"
  "v_xor_b32 v59, 48, v106\n"
  "ds_write_b128 v105, v[68:71] offset:32768\n"
  "s_waitcnt vmcnt(8)\n"
  "v_cmp_gt_u32 vcc, s58, v85\n"
  "v_mov_b32 v100, 0x3f803f80\n"
  "v_mov_b32 v101, 0x3f803f80\n"
  "v_mov_b32 v102, 0x3f803f80\n"
  "v_mov_b32 v103, 0x3f803f80\n"
  "s_and_b32 s68, vcc_lo, s59\n"
  "s_cselect_b32 s61, 0, 1\n"
  "s_cmp_lt_u32 s60, 62\n"
  "s_cselect_b32 s61, s61, 0\n"
  "s_cmp_eq_u32 s61, 0\n"
  "s_waitcnt lgkmcnt(0)\n"
  "s_barrier\n"
  "ds_read_b128 v[0:3], v24 offset:36864\n"
  "ds_read_b128 v[4:7], v25 offset:36864\n"
  "ds_read_b128 v[8:11], v26 offset:36864\n"
  "ds_read_b128 v[12:15], v24 offset:40960\n"
  "ds_read_b128 v[16:19], v25 offset:40960\n"
  "ds_read_b128 v[20:23], v26 offset:40960\n"
  "ds_read_b128 v[32:35], v24 offset:45056\n"
  "ds_read_b128 v[36:39], v25 offset:45056\n"
  "ds_read_b128 v[40:43], v26 offset:45056\n"
  "ds_read_b128 v[44:47], v24 offset:32768\n"
  "ds_read_b128 v[48:51], v25 offset:32768\n"
  "ds_read_b128 v[52:55], v26 offset:32768\n"
  "s_cbranch_scc1 .Lh_nod1_%=\n"
  "s_add_u32 m0, s93, 16384\n"
  "s_add_u32 s67, s92, 16384\n"
  "buffer_load_dwordx4 v106, %[rx], s67 offen offset:0 sc1 lds\n"
  "buffer_load_dwordx4 v57, %[rx], s67 offen offset:1024 sc1 lds\n"
  "buffer_load_dwordx4 v58, %[rx], s67 offen offset:2048 sc1 lds\n"
  "buffer_load_dwordx4 v59, %[rx], s67 offen offset:3072 sc1 lds\n"
  ".Lh_nod1_%=:\n"
  "s_sub_u32 s27, s100, s26\n"
  "s_add_u32 s27, s27, 13\n"
  "s_min_u32 s27, s27, 0x1c0\n"
  "s_add_u32 s60, s60, 2\n"
  "s_waitcnt lgkmcnt(9)\n"
  "v_pk_add_f32 v[4:5], v[4:5], v[8:9]\n"
  "v_pk_add_f32 v[6:7], v[6:7], v[10:11]\n"
  "v_pk_add_f32 v[72:73], v[72:73], v[0:1]\n"
  "v_pk_add_f32 v[74:75], v[74:75], v[2:3]\n"
  "v_pk_add_f32 v[72:73], v[72:73], v[4:5]\n"
  "v_pk_add_f32 v[74:75], v[74:75], v[6:7]\n"
  "s_waitcnt lgkmcnt(6)\n"
  "v_pk_add_f32 v[16:17], v[16:17], v[20:21]\n"
  "v_pk_add_f32 v[18:19], v[18:19], v[22:23]\n"
  "v_pk_add_f32 v[76:77], v[76:77], v[12:13]\n"
  "v_pk_add_f32 v[78:79], v[78:79], v[14:15]\n"
  "v_pk_add_f32 v[76:77], v[76:77], v[16:17]\n"
  "v_pk_add_f32 v[78:79], v[78:79], v[18:19]\n"
  "s_waitcnt lgkmcnt(3)\n"
  "v_pk_add_f32 v[36:37], v[36:37], v[40:41]\n"
  "v_pk_add_f32 v[38:39], v[38:39], v[42:43]\n"
  "v_pk_add_f32 v[80:81], v[80:81], v[32:33]\n"
  "v_pk_add_f32 v[82:83], v[82:83], v[34:35]\n"
  "v_pk_add_f32 v[80:81], v[80:81], v[36:37]\n"
  "v_pk_add_f32 v[82:83], v[82:83], v[38:39]\n"
  "s_waitcnt lgkmcnt(0)\n"
  "s_barrier\n"
  "s_cmp_eq_u32 s61, 0\n"
  "s_cbranch_scc1 .Lh_nod0_%=\n"
  "s_add_u32 m0, s93, 0\n"
  "s_add_u32 s67, s92, 0\n"
  "buffer_load_dwordx4 v106, %[rx], s67 offen offset:0 sc1 lds\n"
  "buffer_load_dwordx4 v57, %[rx], s67 offen offset:1024 sc1 lds\n"
  "buffer_load_dwordx4 v58, %[rx], s67 offen offset:2048 sc1 lds\n"
  "buffer_load_dwordx4 v59, %[rx], s67 offen offset:3072 sc1 lds\n"
  ".Lh_nod0_%=:\n"
  "v_pk_add_f32 v[48:49], v[48:49], v[52:53]\n"
  "v_pk_add_f32 v[50:51], v[50:51], v[54:55]\n"
  "v_pk_add_f32 v[68:69], v[68:69], v[44:45]\n"
  "v_pk_add_f32 v[70:71], v[70:71], v[46:47]\n"
  "v_pk_add_f32 v[68:69], v[68:69], v[48:49]\n"
  "v_pk_add_f32 v[70:71], v[70:71], v[50:51]\n"
  "v_cndmask_b32_e64 v72, v72, v27, s[80:81]\n"
  "v_cndmask_b32_e64 v80, v80, v27, s[80:81]\n"
  "v_cndmask_b32_e64 v73, v73, v27, s[82:83]\n"
  "v_cndmask_b32_e64 v81, v81, v27, s[82:83]\n"
  "v_cndmask_b32_e64 v74, v74, v27, s[84:85]\n"
  "v_cndmask_b32_e64 v82, v82, v27, s[84:85]\n"
  "v_cndmask_b32_e64 v75, v75, v27, s[86:87]\n"
  "v_cndmask_b32_e64 v83, v83, v27, s[86:87]\n"
  "v_max3_f32 v31, v68, v69, v70\n"
  "v_max3_f32 v29, v72, v73, v74\n"
  "v_max_f32 v31, v31, v71\n"
  "v_max3_f32 v30, v76, v77, v78\n"
  "v_cndmask_b32_e64 v0, v28, v31, s[78:79]\n"
  "v_cndmask_b32_e64 v1, v31, v28, s[78:79]\n"
  "v_max3_f32 v30, v30, v79, v80\n"
  "v_max3_f32 v29, v29, v75, v0\n"
  "v_max3_f32 v30, v30, v81, v82\n"
  "v_max3_f32 v30, v30, v83, v1\n"
  "s_nop 1\n"
  "v_permlane16_swap_b32 v29, v30\n"
  "v_max_f32 v29, v29, v30\n"
  "v_mov_b32 v30, v29\n"
  "s_nop 1\n"
  "v_permlane32_swap_b32 v29, v30\n"
  "v_max_f32 v29, v29, v30\n"
  "v_mul_f32 v0, 0xbe0293ee, v29\n"
  "v_mov_b32 v2, v0\n"
  "s_nop 1\n"
  "v_permlane16_swap_b32 v0, v2\n"
  "v_cndmask_b32_e64 v4, v2, v0, s[78:79]\n"
  "v_pk_fma_f32 v[76:77], v[76:77], s[94:95], v[2:3] op_sel_hi:[1,1,0]\n"
  "v_pk_fma_f32 v[78:79], v[78:79], s[94:95], v[2:3] op_sel_hi:[1,1,0]\n"
  "v_pk_fma_f32 v[80:81], v[80:81], s[94:95], v[2:3] op_sel_hi:[1,1,0]\n"
  "v_pk_fma_f32 v[82:83], v[82:83], s[94:95], v[2:3] op_sel_hi:[1,1,0]\n"
  "v_pk_fma_f32 v[72:73], v[72:73], s[94:95], v[0:1] op_sel_hi:[1,1,0]\n"
  "v_pk_fma_f32 v[74:75], v[74:75], s[94:95], v[0:1] op_sel_hi:[1,1,0]\n"
  "v_pk_fma_f32 v[68:69], v[68:69], s[94:95], v[4:5] op_sel_hi:[1,1,0]\n"
  "v_pk_fma_f32 v[70:71], v[70:71], s[94:95], v[4:5] op_sel_hi:[1,1,0]\n"
  "v_exp_f32 v76, v76\n"
  "v_exp_f32 v77, v77\n"
  "v_exp_f32 v78, v78\n"
  "v_exp_f32 v79, v79\n"
  "v_exp_f32 v80, v80\n"
  "v_exp_f32 v81, v81\n"
  "v_exp_f32 v82, v82\n"
  "v_exp_f32 v83, v83\n"
  "v_cvt_pk_bf16_f32 v76, v76, v77\n"
  "v_cvt_pk_bf16_f32 v77, v78, v79\n"
  "v_cvt_pk_bf16_f32 v78, v80, v81\n"
  "v_cvt_pk_bf16_f32 v79, v82, v83\n"
  "v_exp_f32 v72, v72\n"
  "v_mfma_f32_16x16x32_bf16 v[4:7], v[100:103], v[76:79], 0\n"
  "v_exp_f32 v73, v73\n"
  "v_mfma_f32_16x16x32_bf16 v[16:19], v[88:91], v[76:79], 0\n"
  "v_exp_f32 v74, v74\n"
  "v_mfma_f32_16x16x32_bf16 v[20:23], v[92:95], v[76:79], 0\n"
  "v_exp_f32 v75, v75\n"
  "v_exp_f32 v68, v68\n"
  "v_cvt_pk_bf16_f32 v72, v72, v73\n"
  "v_cvt_pk_bf16_f32 v73, v74, v75\n"
  "v_exp_f32 v69, v69\n"
  "v_mfma_f32_16x16x16_bf16 v[0:3], v[100:101], v[72:73], 0\n"
  "v_exp_f32 v70, v70\n"
  "v_mfma_f32_16x16x16_bf16 v[8:11], v[88:89], v[72:73], 0\n"
  "v_exp_f32 v71, v71\n"
  "v_mfma_f32_16x16x16_bf16 v[12:15], v[92:93], v[72:73], 0\n"
  "v_cvt_pk_bf16_f32 v84, v68, v69\n"
  "v_cvt_pk_bf16_f32 v85, v70, v71\n"
  "v_cndmask_b32_e64 v70, v84, 0, s[78:79]\n"
  "v_cndmask_b32_e64 v71, v85, 0, s[78:79]\n"
  "v_cndmask_b32_e64 v68, 0, v84, s[78:79]\n"
  "v_cndmask_b32_e64 v69, 0, v85, s[78:79]\n"
  "v_mfma_f32_16x16x16_bf16 v[4:7], v[100:101], v[70:71], v[4:7]\n"
  "v_mfma_f32_16x16x16_bf16 v[0:3], v[100:101], v[68:69], v[0:3]\n"
  "v_mfma_f32_16x16x16_bf16 v[16:19], v[96:97], v[70:71], v[16:19]\n"
  "v_mfma_f32_16x16x16_bf16 v[20:23], v[98:99], v[70:71], v[20:23]\n"
  "v_rcp_f32 v26, v4\n"
  "v_mfma_f32_16x16x16_bf16 v[8:11], v[96:97], v[68:69], v[8:11]\n"
  "v_rcp_f32 v24, v0\n"
  "v_mfma_f32_16x16x16_bf16 v[12:15], v[98:99], v[68:69], v[12:15]\n"
  "v_pk_mul_f32 v[16:17], v[16:17], v[26:27] op_sel_hi:[1,0]\n"
  "v_pk_mul_f32 v[18:19], v[18:19], v[26:27] op_sel_hi:[1,0]\n"
  "v_pk_mul_f32 v[20:21], v[20:21], v[26:27] op_sel_hi:[1,0]\n"
  "v_pk_mul_f32 v[22:23], v[22:23], v[26:27] op_sel_hi:[1,0]\n"
  "v_cvt_pk_bf16_f32 v16, v16, v17\n"
  "v_cvt_pk_bf16_f32 v17, v18, v19\n"
  "v_cvt_pk_bf16_f32 v18, v20, v21\n"
  "v_cvt_pk_bf16_f32 v19, v22, v23\n"
  "buffer_store_dwordx4 v[16:19], v104, %[rws], s72 offen sc1\n"
  "v_pk_mul_f32 v[8:9], v[8:9], v[24:25] op_sel_hi:[1,0]\n"
  "v_pk_mul_f32 v[10:11], v[10:11], v[24:25] op_sel_hi:[1,0]\n"
  "v_pk_mul_f32 v[12:13], v[12:13], v[24:25] op_sel_hi:[1,0]\n"
  "v_pk_mul_f32 v[14:15], v[14:15], v[24:25] op_sel_hi:[1,0]\n"
  "v_cvt_pk_bf16_f32 v8, v8, v9\n"
  "v_cvt_pk_bf16_f32 v9, v10, v11\n"
  "v_cvt_pk_bf16_f32 v10, v12, v13\n"
  "v_cvt_pk_bf16_f32 v11, v14, v15\n"
  "buffer_store_dwordx4 v[8:11], v104, %[rws], s71 offen sc1\n"
  "s_branch .Lh_top_%=\n"
  ".Lh_cp0_%=:\n"
  "buffer_load_dwordx4 v[24:27], v114, %[rws], s66 offen sc1\n"
  "buffer_load_dwordx4 v[28:31], v114, %[rws], s66 offen offset:256 sc1\n"
  "buffer_load_dwordx4 v[16:19], v114, %[rws], s73 offen sc1\n"
  "buffer_load_dwordx4 v[20:23], v114, %[rws], s73 offen offset:256 sc1\n"
  "s_mov_b32 s63, 1\n"
  "s_add_u32 s29, s100, 0x10000000\n"
  "s_branch .Lh_cp0r_%=\n"
  ".Lh_cp1_%=:\n"
  "buffer_load_dwordx4 v[24:27], v114, %[rws], s66 offen sc1\n"
  "buffer_load_dwordx4 v[28:31], v114, %[rws], s66 offen offset:256 sc1\n"
  "buffer_load_dwordx4 v[16:19], v114, %[rws], s73 offen sc1\n"
  "buffer_load_dwordx4 v[20:23], v114, %[rws], s73 offen offset:256 sc1\n"
  "s_mov_b32 s63, 1\n"
  "s_add_u32 s29, s100, 0x10000000\n"
  "s_branch .Lh_cp1r_%=\n"
  ".Lh_wait_%=:\n"
  "s_cmp_eq_u32 s74, 1\n"
  "s_cbranch_scc1 .Lh_wait4_%=\n"
  "s_cmp_lt_u32 s60, 2\n"
  "s_cbranch_scc1 .Lh_first_%=\n"
  ".Lh_wait0_%=:\n"
  "s_mov_b32 s69, 0\n"
  ".Lh_wait1_%=:\n"
  "buffer_load_dword v32, v122, %[rws], %[huin] offen sc1\n"
  "s_waitcnt vmcnt(0)\n"
  "v_cmp_gt_u32 vcc, s98, v32\n"
  "s_cbranch_vccz .Lh_wait2_%=\n"
  "s_add_u32 s69, s69, 1\n"
  "s_cmp_lt_u32 s69, 262144\n"
  "s_cbranch_scc0 .Lh_abort_%=\n"
  "s_sleep 6\n"
  "s_branch .Lh_wait1_%=\n"
  ".Lh_wait2_%=:\n"
  "s_memrealtime s[100:101]\n"
  "s_waitcnt lgkmcnt(0)\n"
  "v_subrev_u32 v33, s100, v32\n"
  "v_bfe_i32 v33, v33, 0, 16\n"
  "v_cmp_gt_u32 vcc, s98, v32\n"
  "v_readfirstlane_b32 s64, v32\n"
  "v_max_i32_dpp v33, v33, v33 row_ror:8 row_mask:0xf bank_mask:0xf bound_ctrl:1\n"
  "s_cmp_eq_u64 vcc, 0\n"
  "s_cselect_b32 s74, 1, 0\n"
  "v_max_i32_dpp v33, v33, v33 row_ror:4 row_mask:0xf bank_mask:0xf bound_ctrl:1\n"
  "s_lshl_b32 s65, s64, 7\n"
  "s_and_b32 s65, s65, 0x7f800000\n"
  "v_max_i32_dpp v33, v33, v33 row_ror:2 row_mask:0xf bank_mask:0xf bound_ctrl:1\n"
  "s_add_u32 s75, s65, 0xd800000\n"
  "s_cmp_lg_u32 s70, 0\n"
  "v_max_i32_dpp v33, v33, v33 row_ror:1 row_mask:0xf bank_mask:0xf bound_ctrl:1\n"
  "s_cselect_b32 s74, s74, 0\n"
  "v_readlane_b32 s29, v33, 0\n"
  "v_readlane_b32 s67, v33, 16\n"
  "s_max_i32 s29, s29, s67\n"
  "s_cmp_lt_u32 s29, 0x200\n"
  "s_cselect_b32 s29, s29, 0\n"
  "s_cmp_eq_u32 s74, 1\n"
  "s_cselect_b32 s29, s29, 0x10000000\n"
  "s_add_u32 s29, s100, s29\n"
  ".Lh_wait3_%=:\n"
  "s_memrealtime s[100:101]\n"
  ".Lh_wait4_%=:\n"
  "s_waitcnt lgkmcnt(0)\n"
  "s_sub_u32 s67, s29, s100\n"
  "s_cmp_gt_i32 s67, 3\n"
  "s_cbranch_scc1 .Lh_wait3_%=\n"
  "buffer_load_dwordx4 v[24:27], v114, %[rws], s66 offen sc1\n"
  "buffer_load_dwordx4 v[28:31], v114, %[rws], s66 offen offset:256 sc1\n"
  "buffer_load_dwordx4 v[16:19], v114, %[rws], s73 offen sc1\n"
  "buffer_load_dwordx4 v[20:23], v114, %[rws], s73 offen offset:256 sc1\n"
  "s_branch .Lh_gota_%=\n"
  ".Lh_nox_%=:\n"
  "s_nop 7\n"
  "s_nop 1\n"
  "s_waitcnt vmcnt(0)\n"
  "s_cmp_eq_u32 s62, 0\n"
  "s_cbranch_scc1 .Lh_nox2_%=\n"
  "v_mov_b32 v1, 1\n"
  "s_mov_b64 exec, 1\n"
  "ds_add_rtn_u32 v0, v123, v1 offset:16\n"
  "s_mov_b64 exec, -1\n"
  "s_waitcnt lgkmcnt(0)\n"
  "v_readfirstlane_b32 s67, v0\n"
  "s_and_b32 s67, s67, 3\n"
  "s_cmp_lg_u32 s67, 3\n"
  "s_cbranch_scc1 .Lh_nox2_%=\n"
  "v_mov_b32 v1, s90\n"
  "buffer_store_dword v1, off, %[rws], %[cnto] sc1\n"
  ".Lh_nox2_%=:\n"
  "s_cmp_lt_u32 s60, 64\n"
  "s_cbranch_scc1 .Lh_nox3_%=\n"
  "s_endpgm\n"
  ".Lh_nox3_%=:\n"
  "s_mov_b32 s62, 0\n"
  "s_mov_b32 s69, 0\n"
  "v_bfe_u32 v1, v104, 4, 4\n"
  "v_mul_u32_u24 v1, %[xstr], v1\n"
  "s_add_u32 s58, s60, 1\n"
  "s_lshl_b32 s67, s58, 1\n"
  "s_mul_i32 s59, s67, 0x5556\n"
  "s_lshr_b32 s59, s59, 16\n"
  "s_mul_i32 s59, s59, 3\n"
  "s_sub_u32 s67, s67, s59\n"
  "s_lshl_b32 s67, s67, 2\n"
  "s_lshl_b32 s59, 15, s67\n"
  "s_xor_b32 s59, s59, 0xfff\n"
  "s_lshr_b32 s67, %[xstr], 2\n"
  "s_sub_u32 s67, 64, s67\n"
  "s_max_u32 s58, s58, s67\n"
  "s_add_u32 s67, s60, 0\n"
  "s_lshl_b32 s92, s67, 15\n"
  "s_lshl_b32 s93, %[wave], 12\n"
  "s_add_u32 s92, s92, s93\n"
  "s_add_u32 s93, s93, 32768\n"
  "v_xor_b32 v57, 16, v106\n"
  "v_xor_b32 v58, 32, v106\n"
  "v_xor_b32 v59, 48, v106\n"
  ".Lh_nox4_%=:\n"
  "buffer_load_dword v0, v1, %[rws], %[cntx] offen sc1\n"
  "s_waitcnt vmcnt(0)\n"
  "v_cmp_gt_u32 vcc, s58, v0\n"
  "s_nop 1\n"
  "s_and_b32 s67, vcc_lo, s59\n"
  "s_cbranch_scc0 .Lh_nox5_%=\n"
  "s_add_u32 s69, s69, 1\n"
  "s_cmp_lt_u32 s69, 262144\n"
  "s_cbranch_scc0 .Lh_abort_%=\n"
  "s_sleep 1\n"
  "s_branch .Lh_nox4_%=\n"
  ".Lh_nox5_%=:\n"
  "s_add_u32 m0, s93, 16384\n"
  "s_add_u32 s67, s92, 16384\n"
  "buffer_load_dwordx4 v106, %[rx], s67 offen offset:0 sc1 lds\n"
  "buffer_load_dwordx4 v57, %[rx], s67 offen offset:1024 sc1 lds\n"
  "buffer_load_dwordx4 v58, %[rx], s67 offen offset:2048 sc1 lds\n"
  "buffer_load_dwordx4 v59, %[rx], s67 offen offset:3072 sc1 lds\n"
  "s_add_u32 m0, s93, 0\n"
  "s_add_u32 s67, s92, 0\n"
  "buffer_load_dwordx4 v106, %[rx], s67 offen offset:0 sc1 lds\n"
  "buffer_load_dwordx4 v57, %[rx], s67 offen offset:1024 sc1 lds\n"
  "buffer_load_dwordx4 v58, %[rx], s67 offen offset:2048 sc1 lds\n"
  "buffer_load_dwordx4 v59, %[rx], s67 offen offset:3072 sc1 lds\n"
  "s_waitcnt vmcnt(0)\n"
  "s_branch .Lh_x_%=\n"
  ".Lh_first_%=:\n"
  "s_nop 7\n"
  "s_nop 1\n"
  "v_lshrrev_b32 v33, 5, v122\n"
  "s_mov_b32 s69, 0\n"
  "s_lshr_b32 s68, %[huin], 5\n"
  "s_add_u32 s68, s68, 3840\n"
  ".Lh_first1_%=:\n"
  "buffer_load_dword v32, v33, %[rws], s68 offen sc1\n"
  "s_waitcnt vmcnt(0)\n"
  "v_cmp_eq_u32 vcc, 0, v32\n"
  "s_cbranch_vccz .Lh_first2_%=\n"
  "s_add_u32 s69, s69, 1\n"
  "s_cmp_lt_u32 s69, 262144\n"
  "s_cbranch_scc0 .Lh_abort_%=\n"
  "s_sleep 4\n"
  "s_branch .Lh_first1_%=\n"
  ".Lh_first2_%=:\n"
  "v_max_f32_dpp v32, v32, v32 row_ror:8 row_mask:0xf bank_mask:0xf bound_ctrl:1\n"
  "s_nop 1\n"
  "v_max_f32_dpp v32, v32, v32 row_ror:4 row_mask:0xf bank_mask:0xf bound_ctrl:1\n"
  "s_nop 1\n"
  "v_max_f32_dpp v32, v32, v32 row_ror:2 row_mask:0xf bank_mask:0xf bound_ctrl:1\n"
  "s_nop 1\n"
  "v_max_f32_dpp v32, v32, v32 row_ror:1 row_mask:0xf bank_mask:0xf bound_ctrl:1\n"
  "v_mov_b32 v33, v32\n"
  "s_nop 1\n"
  "v_permlane16_swap_b32 v32, v33\n"
  "v_max_f32 v32, v32, v33\n"
  "v_mul_f32 v32, 0x41c80000, v32\n"
  "s_nop 0\n"
  "v_readfirstlane_b32 s70, v32\n"
  "s_cmp_eq_u32 s60, 0\n"
  "s_cbranch_scc0 .Lh_wait0_%=\n"
  "s_nop 7\n"
  "s_nop 1\n"
  "s_branch .Lh_pre_%=\n"
  ".Lh_zero_%=:\n"
  "s_nop 7\n"
  "s_nop 1\n"
  "v_lshlrev_b32 v12, 6, v104\n"
  "v_and_b32 v12, 0x1c00, v12\n"
  "v_lshrrev_b32 v13, 3, v104\n"
  "v_and_b32 v13, 0x60, v13\n"
  "v_add_u32 v12, v12, v13\n"
  "s_lshl_b32 s67, %[wave], 8\n"
  "v_add_u32 v12, s67, v12\n"
  "global_load_dwordx4 v[16:19], v12, %[mem0] offset:0\n"
  "global_load_dwordx4 v[20:23], v12, %[mem0] offset:16\n"
  "global_load_dwordx4 v[24:27], v12, %[mem0] offset:128\n"
  "global_load_dwordx4 v[28:31], v12, %[mem0] offset:144\n"
  "v_mov_b32 v4, 0\n"
  "s_waitcnt vmcnt(3)\n"
  "v_cndmask_b32_e64 v32, v17, v16, s[76:77]\n"
  "v_cndmask_b32_e64 v33, v19, v18, s[76:77]\n"
  "s_waitcnt vmcnt(2)\n"
  "v_cndmask_b32_e64 v34, v21, v20, s[76:77]\n"
  "v_cndmask_b32_e64 v35, v23, v22, s[76:77]\n"
  "s_waitcnt vmcnt(1)\n"
  "v_cndmask_b32_e64 v36, v25, v24, s[76:77]\n"
  "v_cndmask_b32_e64 v37, v27, v26, s[76:77]\n"
  "s_waitcnt vmcnt(0)\n"
  "v_cndmask_b32_e64 v38, v29, v28, s[76:77]\n"
  "v_cndmask_b32_e64 v39, v31, v30, s[76:77]\n"
  "ds_write_b128 v105, v[32:35] offset:32768\n"
  "ds_write_b128 v105, v[36:39] offset:36864\n"
  "v_lshlrev_b32 v12, 16, v32\n"
  "v_and_b32 v13, 0xffff0000, v32\n"
  "v_max3_f32 v4, |v12|, |v13|, v4\n"
  "v_lshlrev_b32 v12, 16, v33\n"
  "v_and_b32 v13, 0xffff0000, v33\n"
  "v_max3_f32 v4, |v12|, |v13|, v4\n"
  "v_lshlrev_b32 v12, 16, v34\n"
  "v_and_b32 v13, 0xffff0000, v34\n"
  "v_max3_f32 v4, |v12|, |v13|, v4\n"
  "v_lshlrev_b32 v12, 16, v35\n"
  "v_and_b32 v13, 0xffff0000, v35\n"
  "v_max3_f32 v4, |v12|, |v13|, v4\n"
  "v_lshlrev_b32 v12, 16, v36\n"
  "v_and_b32 v13, 0xffff0000, v36\n"
  "v_max3_f32 v4, |v12|, |v13|, v4\n"
  "v_lshlrev_b32 v12, 16, v37\n"
  "v_and_b32 v13, 0xffff0000, v37\n"
  "v_max3_f32 v4, |v12|, |v13|, v4\n"
  "v_lshlrev_b32 v12, 16, v38\n"
  "v_and_b32 v13, 0xffff0000, v38\n"
  "v_max3_f32 v4, |v12|, |v13|, v4\n"
  "v_lshlrev_b32 v12, 16, v39\n"
  "v_and_b32 v13, 0xffff0000, v39\n"
  "v_max3_f32 v4, |v12|, |v13|, v4\n"
  "s_waitcnt vmcnt(0) lgkmcnt(0)\n"
  "s_nop 7\n"
  "s_nop 1\n"
  "s_branch .Lh_decd_%=\n"
  ".Lh_retry_%=:\n"
  "s_mov_b32 s69, 0\n"
  ".Lh_retry1_%=:\n"
  "s_add_u32 s69, s69, 1\n"
  "s_cmp_lt_u32 s69, 262144\n"
  "s_cbranch_scc0 .Lh_abort_%=\n"
  "s_sleep 4\n"
  "buffer_load_dwordx4 v[24:27], v114, %[rws], s66 offen sc1\n"
  "buffer_load_dwordx4 v[28:31], v114, %[rws], s66 offen offset:256 sc1\n"
  "buffer_load_dwordx4 v[16:19], v114, %[rws], s73 offen sc1\n"
  "buffer_load_dwordx4 v[20:23], v114, %[rws], s73 offen offset:256 sc1\n"
  "s_waitcnt vmcnt(2)\n"
  "v_cvt_f32_i32 v24, v24\n"
  "v_cvt_f32_i32 v25, v25\n"
  "v_cvt_f32_i32 v26, v26\n"
  "v_cvt_f32_i32 v27, v27\n"
  "v_cvt_f32_i32 v28, v28\n"
  "v_cvt_f32_i32 v29, v29\n"
  "v_cvt_f32_i32 v30, v30\n"
  "v_cvt_f32_i32 v31, v31\n"
  "v_pk_mul_f32 v[24:25], v[24:25], s[64:65] op_sel:[0,1] op_sel_hi:[1,1]\n"
  "v_pk_mul_f32 v[26:27], v[26:27], s[64:65] op_sel:[0,1] op_sel_hi:[1,1]\n"
  "v_pk_mul_f32 v[28:29], v[28:29], s[64:65] op_sel:[0,1] op_sel_hi:[1,1]\n"
  "v_pk_mul_f32 v[30:31], v[30:31], s[64:65] op_sel:[0,1] op_sel_hi:[1,1]\n"
  "v_cvt_pk_bf16_f32 v32, v24, v25\n"
  "v_cvt_pk_bf16_f32 v33, v26, v27\n"
  "v_cvt_pk_bf16_f32 v34, v28, v29\n"
  "v_cvt_pk_bf16_f32 v35, v30, v31\n"
  "ds_write_b128 v105, v[32:35] offset:32768\n"
  "s_waitcnt vmcnt(0)\n"
  "v_cvt_f32_i32 v16, v16\n"
  "v_cvt_f32_i32 v17, v17\n"
  "v_cvt_f32_i32 v18, v18\n"
  "v_cvt_f32_i32 v19, v19\n"
  "v_cvt_f32_i32 v20, v20\n"
  "v_cvt_f32_i32 v21, v21\n"
  "v_cvt_f32_i32 v22, v22\n"
  "v_cvt_f32_i32 v23, v23\n"
  "v_pk_mul_f32 v[16:17], v[16:17], s[64:65] op_sel:[0,1] op_sel_hi:[1,1]\n"
  "v_pk_mul_f32 v[18:19], v[18:19], s[64:65] op_sel:[0,1] op_sel_hi:[1,1]\n"
  "v_pk_mul_f32 v[20:21], v[20:21], s[64:65] op_sel:[0,1] op_sel_hi:[1,1]\n"
  "v_pk_mul_f32 v[22:23], v[22:23], s[64:65] op_sel:[0,1] op_sel_hi:[1,1]\n"
  "v_cvt_pk_bf16_f32 v36, v16, v17\n"
  "v_cvt_pk_bf16_f32 v37, v18, v19\n"
  "v_cvt_pk_bf16_f32 v38, v20, v21\n"
  "v_cvt_pk_bf16_f32 v39, v22, v23\n"
  "ds_write_b128 v105, v[36:39] offset:36864\n"
  "v_max3_f32 v4, |v16|, |v17|, |v18|\n"
  "v_max3_f32 v5, |v19|, |v20|, |v21|\n"
  "v_max3_f32 v6, |v22|, |v23|, |v24|\n"
  "v_max3_f32 v7, |v25|, |v26|, |v27|\n"
  "v_max3_f32 v4, v4, |v28|, |v29|\n"
  "v_max3_f32 v5, v5, |v30|, |v31|\n"
  "v_max3_f32 v4, v4, v5, v6\n"
  "v_max_f32 v4, v4, v7\n"
  "v_cmp_le_f32 vcc, s75, v4\n"
  "s_cbranch_vccnz .Lh_retry1_%=\n"
  "s_branch .Lh_decd_%=\n"
  ".Lh_max_%=:\n"
  "ds_max_u32 v123, v4\n"
  "s_branch .Lh_max_r_%=\n"
  ".Lh_abort_%=:\n"
  "v_mov_b32 v0, 1\n"
  "buffer_store_dword v0, off, %[rws], 0 offset:2048 sc1\n"
  "s_endpgm\n"
      :
      : [rws] "s"(rws), [rx] "s"(rx), "{s60}"(rep), [wave] "s"(wave), [wo] "s"(WO), [mem0] "s"(p.mem0),
        [cntx] "s"(layer == 0 ? OFF_CNTS : OFF_PD), [xstr] "s"(layer == 0 ? 0 : PST), [cnto] "s"(OFF_PH + (grp * 32 + h) * PST),
        [huin] "s"(OFF_HU + grp * 4096), [huout] "s"(wave == 0 ? OFF_HU + ((grp ^ 1) * 32 + h) * 128 : OFF_PH + (grp * 32 + h) * PST + 128),
        [rot] "s"((h & 3) << 11),
        [obase] "s"(OFF_O + (layer * NUNIT * 128 + 4 * h + wave) * 1024),
        [prof] "s"(OFF_PROF + (((role * 4 + wave) * 129) << 7) - 256),
        "{v120}"(csv0), "{v121}"(csv1), "{v[124:127]}"(wo_last)
      : "memory", "vcc", "scc", "v0","v1","v2","v3","v4","v5","v6","v7","v8","v9","v10","v11","v12","v13","v14","v15","v16","v17","v18","v19","v20","v21","v22","v23","v24","v25","v26","v27","v28","v29","v30","v31","v32","v33","v34","v35","v36","v37","v38","v39","v40","v41","v42","v43","v44","v45","v46","v47","v48","v49","v50","v51","v52","v53","v54","v55","v56","v57","v58","v59","v60","v61","v62","v63","v64","v65","v66","v67","v68","v69","v70","v71","v72","v73","v74","v75","v76","v77","v78","v79","v80","v81","v82","v83","v84","v85","v86","v87","v88","v89","v90","v91","v92","v93","v94","v95","v96","v97","v98","v99","v100","v101","v102","v103","v104","v105","v106","v107","v108","v109","v110","v111","v112","v113","v114","v115","v116","v117","v118","v119","v122","v123",
        "s26","s27","s28","s29","s30","s31","s56","s57","s58","s59","s61","s62","s63","s64","s65","s66","s67","s68","s69","s70","s71","s72","s73","s74","s75","s76","s77","s78","s79","s80","s81","s82","s83","s84","s85","s86","s87","s88","s89","s90","s92","s93","s94","s95","s98","s99","s100","s101" HPROFCLOB);
  __builtin_unreachable();
}

#define SCLOB "s24","s25","s32","s33","s34","s35","s36","s37","s38","s39","s40","s41","s61","s62","s63","s64","s65","s66","s67","s68","s69","s70","s71","s72","s73","s74","s75","s76","s77","s78","s79"
#define VCLOB "v0","v1","v2","v3","v4","v5","v6","v7","v8","v9","v10","v11","v12","v13","v14","v15","v16","v17","v18","v19","v20","v21","v22","v23","v24","v25","v26","v27","v28","v29","v30","v31","v32","v33","v34","v35","v36","v37","v38","v39","v40","v41","v42","v43","v44","v45","v46","v47","v48","v49","v50","v51","v52","v53","v54","v55","v56","v57","v58","v59","v60","v61","v62","v63","v64","v65","v66","v67","v68","v69","v70","v71","v72","v73","v74","v75","v76","v77","v78","v79","v80","v81","v82","v83","v84","v85","v86","v87","v88","v89","v90","v91","v92","v93","v94","v95","v96","v97","v98","v99","v100","v101","v102","v103","v104","v105","v106","v107","v108","v109","v110","v111","v112","v113","v114","v115","v116","v117","v118","v119","v120","v121","v122","v123","v124","v125","v126","v127"
DI void role_W(const Params& p, const int role, const int wave, const int layer, const int rep, const int cb) {
  constexpr int KR = 24, KL = 8, KB = 4096, NSLOT = 10;
  const int lane = lane_id(), g = lane >> 4;
  const rsrc_t rws = mkrsrc(p.ws, WS_USED);
  int pidx = 128; (void)pidx;
  unsigned pv = 0; (void)pv;
  STAMPR(); STAMP(0);
  const unsigned WL = (unsigned)wave * (unsigned)(KL * KB);
  const char* src = (const char*)p.wo + ((size_t)wave * 1024 * 512 + cb * 64) * 2;
  const int q = (lane >> 2) & 3, pp = lane & 3, lrow = lane >> 3;
  const unsigned voff = (unsigned)lrow * 1024u + (unsigned)(((lane & 7) ^ (((lrow >> 1) & 1) << 2)) << 4);
  const unsigned ring = (unsigned)wave * 40960u;
  const unsigned t0 = (unsigned)(8 * g + q) * 128u + (unsigned)((((q >> 1) << 2) | pp) << 4), t1 = t0 ^ 64u;
  auto issue = [&](int ks, unsigned dst) {
#pragma unroll
    for (int i = 0; i < 4; ++i) glds16(src + (size_t)(ks * 32 + i * 8) * 1024, voff, dst + (unsigned)i * 1024u);
  };
#pragma unroll
  for (int ks = 0; ks < NSLOT; ++ks) issue(ks, ring + (unsigned)ks * KB);
  sfor<KR>([&](auto KSI) {
    constexpr int ks = KSI;
    wait_vm<((ks + NSLOT <= KR ? NSLOT : KR - ks) - 1) * 4>();
    sfor<4>([&](auto N) { constexpr int n = N; tr_slot<ks * 4 + n, (ks % NSLOT) * KB + 8 * (n & 1), 512>(ring + ((n >> 1) ? t1 : t0)); });
    WAIT_LDS();
    if (ks + NSLOT < KR) issue(ks + NSLOT, ring + (unsigned)(ks % NSLOT) * KB);
  });
  BAR();
#pragma unroll
  for (int i = 0; i < KL; ++i) issue(KR + i, WL + (unsigned)i * KB);
  wait_vm<0>();
#pragma unroll
  for (int i = 0; i < KL; i += 2) {
    const unsigned ta[4] = {WL + i * KB + t0, WL + i * KB + t1, WL + (i + 1) * KB + t0, WL + (i + 1) * KB + t1};
    v4u f[8];
    tr_oct<0, 512>(ta, f);
#pragma unroll
    for (int n = 0; n < 8; ++n) sts128(WL + (unsigned)lane * 16u, i * KB + n * 1024, f[n]);
  }
  BAR();
  STAMP(1); FLUSH();
  const rsrc_t rres = layer == 0 ? mkrsrc(p.x, 2048 * 1024) : mkrsrc(p.ws + OFF_X2, 2048 * 1024);
  const rsrc_t rdst = mkrsrc(p.ws + OFF_X1 + layer * (2 << 20), 2048 * 1024);
  asm volatile(
  "v_mbcnt_lo_u32_b32 v0, -1, 0\n"
  "v_mbcnt_hi_u32_b32 v0, -1, v0\n"
  "v_and_b32 v1, 15, v0\n"
  "v_lshrrev_b32 v2, 4, v0\n"
  "v_lshlrev_b32 v120, 4, v0\n"
  "v_cmp_gt_u32 vcc, 8, v1\n"
  "v_lshlrev_b32 v121, 8, v1\n"
  "s_nop 0\n"
  "v_cndmask_b32_e64 v121, 0, v121, vcc\n"
  "v_bfe_u32 v3, v0, 3, 2\n"
  "s_lshl_b32 s61, %[wave], 2\n"
  "v_add_u32 v3, s61, v3\n"
  "v_and_b32 v4, 7, v0\n"
  "v_lshlrev_b32 v6, 4, v4\n"
  "v_lshl_add_u32 v6, v3, 10, v6\n"
  "v_add_u32 v6, %[col], v6\n"
  "v_mov_b32 v7, 0x7ff00000\n"
  "v_cmp_gt_u32 vcc, 32, v0\n"
  "s_nop 1\n"
  "v_cndmask_b32_e64 v122, v7, v6, vcc\n"
  "v_lshlrev_b32 v6, 4, v3\n"
  "v_and_b32 v5, 39, v0\n"
  "v_cmp_eq_u32 vcc, 0, v5\n"
  "s_nop 1\n"
  "v_cndmask_b32_e64 v123, v7, v6, vcc\n"
  "s_lshl_b32 s61, %[wave], 15\n"
  "v_add_u32 v124, s61, v120\n"
  "v_mov_b32 v125, 0x3f803f80\n"
  "s_lshl_b32 s61, %[wave], 12\n"
  "s_add_u32 s61, s61, 131072\n"
  "v_lshl_add_u32 v6, v2, 2, v1\n"
  "v_and_b32 v6, 15, v6\n"
  "v_lshl_add_u32 v6, v2, 4, v6\n"
  "v_lshl_add_u32 v126, v6, 4, s61\n"
  "v_lshrrev_b32 v6, 2, v4\n"
  "v_lshlrev_b32 v6, 11, v6\n"
  "v_and_b32 v5, 3, v4\n"
  "v_lshl_add_u32 v6, v5, 8, v6\n"
  "v_lshl_add_u32 v5, v5, 2, v3\n"
  "v_and_b32 v5, 15, v5\n"
  "v_lshl_add_u32 v6, v5, 4, v6\n"
  "v_add_u32 v127, 131072, v6\n"
  "s_lshr_b32 s66, s60, 1\n"
  "s_and_b32 s65, s66, 1\n"
  "s_lshl_b32 s65, s65, 13\n"
  "s_add_u32 s65, s65, %[pin]\n"
  "s_add_u32 s66, s66, 1\n"
  "s_lshl_b32 s70, s60, 17\n"
  "s_add_u32 s70, s70, %[obase]\n"
  "s_lshl_b32 s71, s60, 14\n"
  "s_lshl_b32 s72, s60, 11\n"
  "s_add_u32 s72, s72, %[stoff]\n"
  "s_add_u32 s73, s70, 4096\n"
  "s_add_u32 s74, s70, 8192\n"
  "s_add_u32 s75, s70, 12288\n"
  "s_add_u32 s76, s70, 16384\n"
  "s_add_u32 s77, s70, 20480\n"
  "s_add_u32 s78, s70, 24576\n"
  "s_add_u32 s79, s70, 28672\n"
  ".Lw_top_%=:\n"
  "s_mov_b32 s64, 0\n"
  ".Lw_poll_%=:\n"
  "buffer_load_dword v0, v121, %[rws], s65 offen sc1\n"
  "s_waitcnt vmcnt(0)\n"
  "v_cmp_gt_u32 vcc, s66, v0\n"
  "s_cbranch_vccnz .Lw_spin_%=\n"
  "buffer_load_dwordx4 v[0:3], v120, %[rws], s70 offen offset:0 sc1\n"
  "buffer_load_dwordx4 v[4:7], v120, %[rws], s70 offen offset:1024 sc1\n"
  "buffer_load_dwordx4 v[8:11], v120, %[rws], s70 offen offset:2048 sc1\n"
  "buffer_load_dwordx4 v[12:15], v120, %[rws], s70 offen offset:3072 sc1\n"
  "buffer_load_dwordx4 v[16:19], v120, %[rws], s73 offen offset:0 sc1\n"
  "buffer_load_dwordx4 v[20:23], v120, %[rws], s73 offen offset:1024 sc1\n"
  "buffer_load_dwordx4 v[24:27], v120, %[rws], s73 offen offset:2048 sc1\n"
  "buffer_load_dwordx4 v[28:31], v120, %[rws], s73 offen offset:3072 sc1\n"
  "buffer_load_dwordx4 v[32:35], v120, %[rws], s74 offen offset:0 sc1\n"
  "buffer_load_dwordx4 v[36:39], v120, %[rws], s74 offen offset:1024 sc1\n"
  "buffer_load_dwordx4 v[40:43], v120, %[rws], s74 offen offset:2048 sc1\n"
  "buffer_load_dwordx4 v[44:47], v120, %[rws], s74 offen offset:3072 sc1\n"
  "buffer_load_dwordx4 v[48:51], v120, %[rws], s75 offen offset:0 sc1\n"
  "buffer_load_dwordx4 v[52:55], v120, %[rws], s75 offen offset:1024 sc1\n"
  "buffer_load_dwordx4 v[56:59], v120, %[rws], s75 offen offset:2048 sc1\n"
  "buffer_load_dwordx4 v[60:63], v120, %[rws], s75 offen offset:3072 sc1\n"
  "buffer_load_dwordx4 v[64:67], v120, %[rws], s76 offen offset:0 sc1\n"
  "buffer_load_dwordx4 v[68:71], v120, %[rws], s76 offen offset:1024 sc1\n"
  "buffer_load_dwordx4 v[72:75], v120, %[rws], s76 offen offset:2048 sc1\n"
  "buffer_load_dwordx4 v[76:79], v120, %[rws], s76 offen offset:3072 sc1\n"
  "s_waitcnt vmcnt(19)\n"
  "v_mfma_f32_16x16x32_bf16 v[80:83], a[0:3], v[0:3], 0\n"
  "v_mfma_f32_16x16x32_bf16 v[84:87], a[4:7], v[0:3], 0\n"
  "v_mfma_f32_16x16x32_bf16 v[88:91], a[8:11], v[0:3], 0\n"
  "v_mfma_f32_16x16x32_bf16 v[92:95], a[12:15], v[0:3], 0\n"
  "buffer_load_dwordx4 v[0:3], v120, %[rws], s77 offen offset:0 sc1\n"
  "s_waitcnt vmcnt(19)\n"
  "v_mfma_f32_16x16x32_bf16 v[80:83], a[16:19], v[4:7], v[80:83]\n"
  "v_mfma_f32_16x16x32_bf16 v[84:87], a[20:23], v[4:7], v[84:87]\n"
  "v_mfma_f32_16x16x32_bf16 v[88:91], a[24:27], v[4:7], v[88:91]\n"
  "v_mfma_f32_16x16x32_bf16 v[92:95], a[28:31], v[4:7], v[92:95]\n"
  "buffer_load_dwordx4 v[4:7], v120, %[rws], s77 offen offset:1024 sc1\n"
  "s_waitcnt vmcnt(19)\n"
  "v_mfma_f32_16x16x32_bf16 v[80:83], a[32:35], v[8:11], v[80:83]\n"
  "v_mfma_f32_16x16x32_bf16 v[84:87], a[36:39], v[8:11], v[84:87]\n"
  "v_mfma_f32_16x16x32_bf16 v[88:91], a[40:43], v[8:11], v[88:91]\n"
  "v_mfma_f32_16x16x32_bf16 v[92:95], a[44:47], v[8:11], v[92:95]\n"
  "buffer_load_dwordx4 v[8:11], v120, %[rws], s77 offen offset:2048 sc1\n"
  "s_waitcnt vmcnt(19)\n"
  "v_mfma_f32_16x16x32_bf16 v[80:83], a[48:51], v[12:15], v[80:83]\n"
  "v_mfma_f32_16x16x32_bf16 v[84:87], a[52:55], v[12:15], v[84:87]\n"
  "v_mfma_f32_16x16x32_bf16 v[88:91], a[56:59], v[12:15], v[88:91]\n"
  "v_mfma_f32_16x16x32_bf16 v[92:95], a[60:63], v[12:15], v[92:95]\n"
  "buffer_load_dwordx4 v[12:15], v120, %[rws], s77 offen offset:3072 sc1\n"
  "s_waitcnt vmcnt(19)\n"
  "v_mfma_f32_16x16x32_bf16 v[80:83], a[64:67], v[16:19], v[80:83]\n"
  "v_mfma_f32_16x16x32_bf16 v[84:87], a[68:71], v[16:19], v[84:87]\n"
  "v_mfma_f32_16x16x32_bf16 v[88:91], a[72:75], v[16:19], v[88:91]\n"
  "v_mfma_f32_16x16x32_bf16 v[92:95], a[76:79], v[16:19], v[92:95]\n"
  "buffer_load_dwordx4 v[16:19], v120, %[rws], s78 offen offset:0 sc1\n"
  "s_waitcnt vmcnt(19)\n"
  "v_mfma_f32_16x16x32_bf16 v[80:83], a[80:83], v[20:23], v[80:83]\n"
  "v_mfma_f32_16x16x32_bf16 v[84:87], a[84:87], v[20:23], v[84:87]\n"
  "v_mfma_f32_16x16x32_bf16 v[88:91], a[88:91], v[20:23], v[88:91]\n"
  "v_mfma_f32_16x16x32_bf16 v[92:95], a[92:95], v[20:23], v[92:95]\n"
  "buffer_load_dwordx4 v[20:23], v120, %[rws], s78 offen offset:1024 sc1\n"
  "s_waitcnt vmcnt(19)\n"
  "v_mfma_f32_16x16x32_bf16 v[80:83], a[96:99], v[24:27], v[80:83]\n"
  "v_mfma_f32_16x16x32_bf16 v[84:87], a[100:103], v[24:27], v[84:87]\n"
  "v_mfma_f32_16x16x32_bf16 v[88:91], a[104:107], v[24:27], v[88:91]\n"
  "v_mfma_f32_16x16x32_bf16 v[92:95], a[108:111], v[24:27], v[92:95]\n"
  "buffer_load_dwordx4 v[24:27], v120, %[rws], s78 offen offset:2048 sc1\n"
  "s_waitcnt vmcnt(19)\n"
  "v_mfma_f32_16x16x32_bf16 v[80:83], a[112:115], v[28:31], v[80:83]\n"
  "v_mfma_f32_16x16x32_bf16 v[84:87], a[116:119], v[28:31], v[84:87]\n"
  "v_mfma_f32_16x16x32_bf16 v[88:91], a[120:123], v[28:31], v[88:91]\n"
  "v_mfma_f32_16x16x32_bf16 v[92:95], a[124:127], v[28:31], v[92:95]\n"
  "buffer_load_dwordx4 v[28:31], v120, %[rws], s78 offen offset:3072 sc1\n"
  "s_waitcnt vmcnt(19)\n"
  "v_mfma_f32_16x16x32_bf16 v[80:83], a[128:131], v[32:35], v[80:83]\n"
  "v_mfma_f32_16x16x32_bf16 v[84:87], a[132:135], v[32:35], v[84:87]\n"
  "v_mfma_f32_16x16x32_bf16 v[88:91], a[136:139], v[32:35], v[88:91]\n"
  "v_mfma_f32_16x16x32_bf16 v[92:95], a[140:143], v[32:35], v[92:95]\n"
  "buffer_load_dwordx4 v[32:35], v120, %[rws], s79 offen offset:0 sc1\n"
  "s_waitcnt vmcnt(19)\n"
  "v_mfma_f32_16x16x32_bf16 v[80:83], a[144:147], v[36:39], v[80:83]\n"
  "v_mfma_f32_16x16x32_bf16 v[84:87], a[148:151], v[36:39], v[84:87]\n"
  "v_mfma_f32_16x16x32_bf16 v[88:91], a[152:155], v[36:39], v[88:91]\n"
  "v_mfma_f32_16x16x32_bf16 v[92:95], a[156:159], v[36:39], v[92:95]\n"
  "buffer_load_dwordx4 v[36:39], v120, %[rws], s79 offen offset:1024 sc1\n"
  "s_waitcnt vmcnt(19)\n"
  "v_mfma_f32_16x16x32_bf16 v[80:83], a[160:163], v[40:43], v[80:83]\n"
  "v_mfma_f32_16x16x32_bf16 v[84:87], a[164:167], v[40:43], v[84:87]\n"
  "v_mfma_f32_16x16x32_bf16 v[88:91], a[168:171], v[40:43], v[88:91]\n"
  "v_mfma_f32_16x16x32_bf16 v[92:95], a[172:175], v[40:43], v[92:95]\n"
  "buffer_load_dwordx4 v[40:43], v120, %[rws], s79 offen offset:2048 sc1\n"
  "s_waitcnt vmcnt(19)\n"
  "v_mfma_f32_16x16x32_bf16 v[80:83], a[176:179], v[44:47], v[80:83]\n"
  "v_mfma_f32_16x16x32_bf16 v[84:87], a[180:183], v[44:47], v[84:87]\n"
  "v_mfma_f32_16x16x32_bf16 v[88:91], a[184:187], v[44:47], v[88:91]\n"
  "v_mfma_f32_16x16x32_bf16 v[92:95], a[188:191], v[44:47], v[92:95]\n"
  "buffer_load_dwordx4 v[44:47], v120, %[rws], s79 offen offset:3072 sc1\n"
  "s_waitcnt vmcnt(19)\n"
  "v_mfma_f32_16x16x32_bf16 v[80:83], a[192:195], v[48:51], v[80:83]\n"
  "v_mfma_f32_16x16x32_bf16 v[84:87], a[196:199], v[48:51], v[84:87]\n"
  "v_mfma_f32_16x16x32_bf16 v[88:91], a[200:203], v[48:51], v[88:91]\n"
  "v_mfma_f32_16x16x32_bf16 v[92:95], a[204:207], v[48:51], v[92:95]\n"
  "buffer_load_dwordx4 v[96:99], v122, %[rres], s71 offen sc1\n"
  "s_waitcnt vmcnt(19)\n"
  "v_mfma_f32_16x16x32_bf16 v[80:83], a[208:211], v[52:55], v[80:83]\n"
  "v_mfma_f32_16x16x32_bf16 v[84:87], a[212:215], v[52:55], v[84:87]\n"
  "v_mfma_f32_16x16x32_bf16 v[88:91], a[216:219], v[52:55], v[88:91]\n"
  "v_mfma_f32_16x16x32_bf16 v[92:95], a[220:223], v[52:55], v[92:95]\n"
  "s_waitcnt vmcnt(18)\n"
  "v_mfma_f32_16x16x32_bf16 v[80:83], a[224:227], v[56:59], v[80:83]\n"
  "v_mfma_f32_16x16x32_bf16 v[84:87], a[228:231], v[56:59], v[84:87]\n"
  "v_mfma_f32_16x16x32_bf16 v[88:91], a[232:235], v[56:59], v[88:91]\n"
  "v_mfma_f32_16x16x32_bf16 v[92:95], a[236:239], v[56:59], v[92:95]\n"
  "s_waitcnt vmcnt(17)\n"
  "v_mfma_f32_16x16x32_bf16 v[80:83], a[240:243], v[60:63], v[80:83]\n"
  "v_mfma_f32_16x16x32_bf16 v[84:87], a[244:247], v[60:63], v[84:87]\n"
  "v_mfma_f32_16x16x32_bf16 v[88:91], a[248:251], v[60:63], v[88:91]\n"
  "v_mfma_f32_16x16x32_bf16 v[92:95], a[252:255], v[60:63], v[92:95]\n"
  "s_waitcnt vmcnt(16)\n"
  "v_mfma_f32_16x16x32_bf16 v[80:83], v[128:131], v[64:67], v[80:83]\n"
  "v_mfma_f32_16x16x32_bf16 v[84:87], v[132:135], v[64:67], v[84:87]\n"
  "v_mfma_f32_16x16x32_bf16 v[88:91], v[136:139], v[64:67], v[88:91]\n"
  "v_mfma_f32_16x16x32_bf16 v[92:95], v[140:143], v[64:67], v[92:95]\n"
  "s_waitcnt vmcnt(15)\n"
  "v_mfma_f32_16x16x32_bf16 v[80:83], v[144:147], v[68:71], v[80:83]\n"
  "v_mfma_f32_16x16x32_bf16 v[84:87], v[148:151], v[68:71], v[84:87]\n"
  "v_mfma_f32_16x16x32_bf16 v[88:91], v[152:155], v[68:71], v[88:91]\n"
  "v_mfma_f32_16x16x32_bf16 v[92:95], v[156:159], v[68:71], v[92:95]\n"
  "s_waitcnt vmcnt(14)\n"
  "v_mfma_f32_16x16x32_bf16 v[80:83], v[160:163], v[72:75], v[80:83]\n"
  "v_mfma_f32_16x16x32_bf16 v[84:87], v[164:167], v[72:75], v[84:87]\n"
  "v_mfma_f32_16x16x32_bf16 v[88:91], v[168:171], v[72:75], v[88:91]\n"
  "v_mfma_f32_16x16x32_bf16 v[92:95], v[172:175], v[72:75], v[92:95]\n"
  "s_waitcnt vmcnt(13)\n"
  "v_mfma_f32_16x16x32_bf16 v[80:83], v[176:179], v[76:79], v[80:83]\n"
  "v_mfma_f32_16x16x32_bf16 v[84:87], v[180:183], v[76:79], v[84:87]\n"
  "v_mfma_f32_16x16x32_bf16 v[88:91], v[184:187], v[76:79], v[88:91]\n"
  "v_mfma_f32_16x16x32_bf16 v[92:95], v[188:191], v[76:79], v[92:95]\n"
  "s_waitcnt vmcnt(12)\n"
  "v_mfma_f32_16x16x32_bf16 v[80:83], v[192:195], v[0:3], v[80:83]\n"
  "v_mfma_f32_16x16x32_bf16 v[84:87], v[196:199], v[0:3], v[84:87]\n"
  "v_mfma_f32_16x16x32_bf16 v[88:91], v[200:203], v[0:3], v[88:91]\n"
  "v_mfma_f32_16x16x32_bf16 v[92:95], v[204:207], v[0:3], v[92:95]\n"
  "s_waitcnt vmcnt(11)\n"
  "v_mfma_f32_16x16x32_bf16 v[80:83], v[208:211], v[4:7], v[80:83]\n"
  "v_mfma_f32_16x16x32_bf16 v[84:87], v[212:215], v[4:7], v[84:87]\n"
  "v_mfma_f32_16x16x32_bf16 v[88:91], v[216:219], v[4:7], v[88:91]\n"
  "v_mfma_f32_16x16x32_bf16 v[92:95], v[220:223], v[4:7], v[92:95]\n"
  "s_waitcnt vmcnt(10)\n"
  "v_mfma_f32_16x16x32_bf16 v[80:83], v[224:227], v[8:11], v[80:83]\n"
  "ds_read_b128 v[112:115], v124 offset:3072\n"
  "v_mfma_f32_16x16x32_bf16 v[84:87], v[228:231], v[8:11], v[84:87]\n"
  "ds_read_b128 v[108:111], v124 offset:2048\n"
  "v_mfma_f32_16x16x32_bf16 v[88:91], v[232:235], v[8:11], v[88:91]\n"
  "ds_read_b128 v[104:107], v124 offset:1024\n"
  "v_mfma_f32_16x16x32_bf16 v[92:95], v[236:239], v[8:11], v[92:95]\n"
  "ds_read_b128 v[100:103], v124 offset:0\n"
  "s_waitcnt vmcnt(9)\n"
  "v_mfma_f32_16x16x32_bf16 v[80:83], v[240:243], v[12:15], v[80:83]\n"
  "ds_read_b128 v[60:63], v124 offset:7168\n"
  "v_mfma_f32_16x16x32_bf16 v[84:87], v[244:247], v[12:15], v[84:87]\n"
  "ds_read_b128 v[56:59], v124 offset:6144\n"
  "v_mfma_f32_16x16x32_bf16 v[88:91], v[248:251], v[12:15], v[88:91]\n"
  "ds_read_b128 v[52:55], v124 offset:5120\n"
  "v_mfma_f32_16x16x32_bf16 v[92:95], v[252:255], v[12:15], v[92:95]\n"
  "ds_read_b128 v[48:51], v124 offset:4096\n"
  "s_waitcnt vmcnt(8) lgkmcnt(4)\n"
  "v_mfma_f32_16x16x32_bf16 v[80:83], v[100:103], v[16:19], v[80:83]\n"
  "ds_read_b128 v[76:79], v124 offset:11264\n"
  "v_mfma_f32_16x16x32_bf16 v[84:87], v[104:107], v[16:19], v[84:87]\n"
  "ds_read_b128 v[72:75], v124 offset:10240\n"
  "v_mfma_f32_16x16x32_bf16 v[88:91], v[108:111], v[16:19], v[88:91]\n"
  "ds_read_b128 v[68:71], v124 offset:9216\n"
  "v_mfma_f32_16x16x32_bf16 v[92:95], v[112:115], v[16:19], v[92:95]\n"
  "ds_read_b128 v[64:67], v124 offset:8192\n"
  "s_waitcnt vmcnt(7) lgkmcnt(4)\n"
  "v_mfma_f32_16x16x32_bf16 v[80:83], v[48:51], v[20:23], v[80:83]\n"
  "ds_read_b128 v[112:115], v124 offset:15360\n"
  "v_mfma_f32_16x16x32_bf16 v[84:87], v[52:55], v[20:23], v[84:87]\n"
  "ds_read_b128 v[108:111], v124 offset:14336\n"
  "v_mfma_f32_16x16x32_bf16 v[88:91], v[56:59], v[20:23], v[88:91]\n"
  "ds_read_b128 v[104:107], v124 offset:13312\n"
  "v_mfma_f32_16x16x32_bf16 v[92:95], v[60:63], v[20:23], v[92:95]\n"
  "ds_read_b128 v[100:103], v124 offset:12288\n"
  "s_waitcnt vmcnt(6) lgkmcnt(4)\n"
  "v_mfma_f32_16x16x32_bf16 v[80:83], v[64:67], v[24:27], v[80:83]\n"
  "ds_read_b128 v[60:63], v124 offset:19456\n"
  "v_mfma_f32_16x16x32_bf16 v[84:87], v[68:71], v[24:27], v[84:87]\n"
  "ds_read_b128 v[56:59], v124 offset:18432\n"
  "v_mfma_f32_16x16x32_bf16 v[88:91], v[72:75], v[24:27], v[88:91]\n"
  "ds_read_b128 v[52:55], v124 offset:17408\n"
  "v_mfma_f32_16x16x32_bf16 v[92:95], v[76:79], v[24:27], v[92:95]\n"
  "ds_read_b128 v[48:51], v124 offset:16384\n"
  "s_waitcnt vmcnt(5) lgkmcnt(4)\n"
  "v_mfma_f32_16x16x32_bf16 v[80:83], v[100:103], v[28:31], v[80:83]\n"
  "ds_read_b128 v[76:79], v124 offset:23552\n"
  "v_mfma_f32_16x16x32_bf16 v[84:87], v[104:107], v[28:31], v[84:87]\n"
  "ds_read_b128 v[72:75], v124 offset:22528\n"
  "v_mfma_f32_16x16x32_bf16 v[88:91], v[108:111], v[28:31], v[88:91]\n"
  "ds_read_b128 v[68:71], v124 offset:21504\n"
  "v_mfma_f32_16x16x32_bf16 v[92:95], v[112:115], v[28:31], v[92:95]\n"
  "ds_read_b128 v[64:67], v124 offset:20480\n"
  "s_waitcnt vmcnt(4) lgkmcnt(4)\n"
  "v_mfma_f32_16x16x32_bf16 v[80:83], v[48:51], v[32:35], v[80:83]\n"
  "ds_read_b128 v[112:115], v124 offset:27648\n"
  "v_mfma_f32_16x16x32_bf16 v[84:87], v[52:55], v[32:35], v[84:87]\n"
  "ds_read_b128 v[108:111], v124 offset:26624\n"
  "v_mfma_f32_16x16x32_bf16 v[88:91], v[56:59], v[32:35], v[88:91]\n"
  "ds_read_b128 v[104:107], v124 offset:25600\n"
  "v_mfma_f32_16x16x32_bf16 v[92:95], v[60:63], v[32:35], v[92:95]\n"
  "ds_read_b128 v[100:103], v124 offset:24576\n"
  "s_waitcnt vmcnt(3) lgkmcnt(4)\n"
  "v_mfma_f32_16x16x32_bf16 v[80:83], v[64:67], v[36:39], v[80:83]\n"
  "ds_read_b128 v[60:63], v124 offset:31744\n"
  "v_mfma_f32_16x16x32_bf16 v[84:87], v[68:71], v[36:39], v[84:87]\n"
  "ds_read_b128 v[56:59], v124 offset:30720\n"
  "v_mfma_f32_16x16x32_bf16 v[88:91], v[72:75], v[36:39], v[88:91]\n"
  "ds_read_b128 v[52:55], v124 offset:29696\n"
  "v_mfma_f32_16x16x32_bf16 v[92:95], v[76:79], v[36:39], v[92:95]\n"
  "ds_read_b128 v[48:51], v124 offset:28672\n"
  "s_waitcnt vmcnt(2) lgkmcnt(4)\n"
  "v_mfma_f32_16x16x32_bf16 v[80:83], v[100:103], v[40:43], v[80:83]\n"
  "v_mfma_f32_16x16x32_bf16 v[84:87], v[104:107], v[40:43], v[84:87]\n"
  "v_mfma_f32_16x16x32_bf16 v[88:91], v[108:111], v[40:43], v[88:91]\n"
  "v_mfma_f32_16x16x32_bf16 v[92:95], v[112:115], v[40:43], v[92:95]\n"
  "s_waitcnt vmcnt(1) lgkmcnt(0)\n"
  "v_mfma_f32_16x16x32_bf16 v[80:83], v[48:51], v[44:47], v[80:83]\n"
  "v_mfma_f32_16x16x32_bf16 v[84:87], v[52:55], v[44:47], v[84:87]\n"
  "v_mfma_f32_16x16x32_bf16 v[88:91], v[56:59], v[44:47], v[88:91]\n"
  "v_mfma_f32_16x16x32_bf16 v[92:95], v[60:63], v[44:47], v[92:95]\n"
  "ds_write_b128 v126, v[80:83] offset:0\n"
  "ds_write_b128 v126, v[84:87] offset:1024\n"
  "s_nop 2\n"
  "ds_write_b128 v126, v[88:91] offset:2048\n"
  "s_nop 2\n"
  "ds_write_b128 v126, v[92:95] offset:3072\n"
  "s_waitcnt lgkmcnt(0)\n"
  "s_barrier\n"
  "ds_read_b128 v[0:3], v127 offset:0\n"
  "ds_read_b128 v[4:7], v127 offset:1024\n"
  "ds_read_b128 v[8:11], v127 offset:4096\n"
  "ds_read_b128 v[12:15], v127 offset:5120\n"
  "ds_read_b128 v[16:19], v127 offset:8192\n"
  "ds_read_b128 v[20:23], v127 offset:9216\n"
  "ds_read_b128 v[24:27], v127 offset:12288\n"
  "ds_read_b128 v[28:31], v127 offset:13312\n"
  "s_waitcnt lgkmcnt(5)\n"
  "v_pk_add_f32 v[0:1], v[0:1], v[8:9]\n"
  "v_pk_add_f32 v[2:3], v[2:3], v[10:11]\n"
  "s_waitcnt lgkmcnt(4)\n"
  "v_pk_add_f32 v[4:5], v[4:5], v[12:13]\n"
  "v_pk_add_f32 v[6:7], v[6:7], v[14:15]\n"
  "s_waitcnt lgkmcnt(1)\n"
  "v_pk_add_f32 v[16:17], v[16:17], v[24:25]\n"
  "v_pk_add_f32 v[18:19], v[18:19], v[26:27]\n"
  "s_waitcnt lgkmcnt(0)\n"
  "v_pk_add_f32 v[20:21], v[20:21], v[28:29]\n"
  "v_pk_add_f32 v[22:23], v[22:23], v[30:31]\n"
  "v_pk_add_f32 v[0:1], v[0:1], v[16:17]\n"
  "v_pk_add_f32 v[2:3], v[2:3], v[18:19]\n"
  "v_pk_add_f32 v[4:5], v[4:5], v[20:21]\n"
  "v_pk_add_f32 v[6:7], v[6:7], v[22:23]\n"
  "v_cvt_pk_bf16_f32 v0, v0, v1\n"
  "v_cvt_pk_bf16_f32 v1, v2, v3\n"
  "v_cvt_pk_bf16_f32 v2, v4, v5\n"
  "v_cvt_pk_bf16_f32 v3, v6, v7\n"
  "v_lshlrev_b32 v12, 16, v0\n"
  "v_and_b32 v13, 0xffff0000, v0\n"
  "s_waitcnt vmcnt(0)\n"
  "v_lshlrev_b32 v14, 16, v96\n"
  "v_and_b32 v15, 0xffff0000, v96\n"
  "v_pk_add_f32 v[12:13], v[12:13], v[14:15]\n"
  "s_nop 0\n"
  "v_cvt_pk_bf16_f32 v8, v12, v13\n"
  "v_lshlrev_b32 v12, 16, v1\n"
  "v_and_b32 v13, 0xffff0000, v1\n"
  "v_lshlrev_b32 v14, 16, v97\n"
  "v_and_b32 v15, 0xffff0000, v97\n"
  "v_pk_add_f32 v[12:13], v[12:13], v[14:15]\n"
  "s_nop 0\n"
  "v_cvt_pk_bf16_f32 v9, v12, v13\n"
  "v_lshlrev_b32 v12, 16, v2\n"
  "v_and_b32 v13, 0xffff0000, v2\n"
  "v_lshlrev_b32 v14, 16, v98\n"
  "v_and_b32 v15, 0xffff0000, v98\n"
  "v_pk_add_f32 v[12:13], v[12:13], v[14:15]\n"
  "s_nop 0\n"
  "v_cvt_pk_bf16_f32 v10, v12, v13\n"
  "v_lshlrev_b32 v12, 16, v3\n"
  "v_and_b32 v13, 0xffff0000, v3\n"
  "v_lshlrev_b32 v14, 16, v99\n"
  "v_and_b32 v15, 0xffff0000, v99\n"
  "v_pk_add_f32 v[12:13], v[12:13], v[14:15]\n"
  "s_nop 0\n"
  "v_cvt_pk_bf16_f32 v11, v12, v13\n"
  "buffer_store_dwordx4 v[8:11], v122, %[rdst], s71 offen sc1\n"
  "v_dot2_f32_bf16 v16, v8, v125, 0\n"
  "v_dot2_f32_bf16 v17, v8, v8, 0\n"
  "v_dot2_f32_bf16 v16, v9, v125, v16\n"
  "v_dot2_f32_bf16 v17, v9, v9, v17\n"
  "v_dot2_f32_bf16 v16, v10, v125, v16\n"
  "v_dot2_f32_bf16 v17, v10, v10, v17\n"
  "v_dot2_f32_bf16 v16, v11, v125, v16\n"
  "v_dot2_f32_bf16 v17, v11, v11, v17\n"
  "s_nop 2\n"
  "v_add_f32_dpp v16, v16, v16 quad_perm:[1,0,3,2] row_mask:0xf bank_mask:0xf bound_ctrl:1\n"
  "v_add_f32_dpp v17, v17, v17 quad_perm:[1,0,3,2] row_mask:0xf bank_mask:0xf bound_ctrl:1\n"
  "s_nop 0\n"
  "v_add_f32_dpp v16, v16, v16 quad_perm:[2,3,0,1] row_mask:0xf bank_mask:0xf bound_ctrl:1\n"
  "v_add_f32_dpp v17, v17, v17 quad_perm:[2,3,0,1] row_mask:0xf bank_mask:0xf bound_ctrl:1\n"
  "s_nop 0\n"
  "v_add_f32_dpp v16, v16, v16 row_half_mirror row_mask:0xf bank_mask:0xf bound_ctrl:1\n"
  "v_add_f32_dpp v17, v17, v17 row_half_mirror row_mask:0xf bank_mask:0xf bound_ctrl:1\n"
  "buffer_store_dwordx4 v[16:19], v123, %[rws], s72 offen sc1\n"
  "s_add_u32 s62, s60, 1\n"
  "v_mov_b32 v0, s62\n"
  "s_mov_b32 s63, s60\n"
  "s_add_u32 s60, s60, 3\n"
  "s_lshr_b32 s66, s60, 1\n"
  "s_and_b32 s65, s66, 1\n"
  "s_lshl_b32 s65, s65, 13\n"
  "s_add_u32 s65, s65, %[pin]\n"
  "s_add_u32 s66, s66, 1\n"
  "s_lshl_b32 s70, s60, 17\n"
  "s_add_u32 s70, s70, %[obase]\n"
  "s_lshl_b32 s71, s60, 14\n"
  "s_lshl_b32 s72, s60, 11\n"
  "s_add_u32 s72, s72, %[stoff]\n"
  "s_add_u32 s73, s70, 4096\n"
  "s_add_u32 s74, s70, 8192\n"
  "s_add_u32 s75, s70, 12288\n"
  "s_add_u32 s76, s70, 16384\n"
  "s_add_u32 s77, s70, 20480\n"
  "s_add_u32 s78, s70, 24576\n"
  "s_add_u32 s79, s70, 28672\n"
  "s_waitcnt vmcnt(0)\n"
  "s_barrier\n"
  "buffer_store_dword v0, off, %[rws], %[pout] sc1\n"
  "s_cmp_lt_u32 s60, 128\n"
  "s_cbranch_scc1 .Lw_top_%=\n"
  "s_endpgm\n"
  ".Lw_spin_%=:\n"
  "s_add_u32 s64, s64, 1\n"
  "s_sleep 1\n"
  "s_cmp_lt_u32 s64, 262144\n"
  "s_cbranch_scc1 .Lw_poll_%=\n"
  "v_mov_b32 v0, 1\n"
  "buffer_store_dword v0, off, %[rws], 0 offset:2048 sc1\n"
  "s_endpgm\n"
      :
      : [rws] "s"(rws), [rres] "s"(rres), [rdst] "s"(rdst), "{s60}"(rep), [wave] "s"(wave), [col] "s"(cb * 128),
        [pin] "s"(OFF_PH + (layer * 64 + 8 * wave) * PST), [pout] "s"(OFF_PW + ((layer * 3 + rep) * 8 + cb) * PST + (wave ? 128 : 0)),
        [obase] "s"(OFF_O + layer * NUNIT * 131072 + wave * 32768), [stoff] "s"(OFF_ST1 + layer * 262144 + cb * 256),
        [prof] "s"(OFF_PROF + (((role * 4 + wave) * 129) << 7))
      : "memory", "vcc", "scc", VCLOB, SCLOB);
  __builtin_unreachable();
}

DI void role_D(const Params& p, const int role, const int wave, const int layer, const int rep, const int cb) {
  const int lane = lane_id(), g = lane >> 4;
  const rsrc_t rws = mkrsrc(p.ws, WS_USED);
  int pidx = 128; (void)pidx;
  unsigned pv = 0; (void)pv;
  STAMPR(); STAMP(0);
  const char* src = (const char*)p.wd + cb * 256;
  const int q = (lane >> 2) & 3, pp = lane & 3;
  const unsigned trb = (unsigned)(8 * g + q) * 256u + (unsigned)(((4 * wave + pp) ^ (q << 2)) << 4);
#pragma unroll
  for (int c = 0; c < 4; ++c) chunk_issue(src + (size_t)c * 131072, 1024u, (unsigned)c * 32768u, wave, lane);
  sfor<12>([&](auto C) {
    constexpr int c = C;
    wait_vm<(c <= 10 ? 24 : 16)>();
    BAR();
    if (c + 4 < 14) chunk_issue(src + (size_t)(c + 4) * 131072, 1024u, (unsigned)((c + 4) % 5) * 32768u, wave, lane);
    const unsigned a = trb + (unsigned)(c % 5) * 32768u;
    sfor<8>([&](auto I) { tr_slot<8 * c + I, 8192 * (I >> 1) + 8 * (I & 1), 1024>(a); });
    WAIT_LDS();
  });
  wait_vm<0>();
  BAR();
  {
    v4u f0[8], f1[8];
    const unsigned ta[4] = {trb + 65536u, trb + 65536u + 8192u, trb + 65536u + 16384u, trb + 65536u + 24576u};
    tr_oct<0, 1024>(ta, f0);
    tr_oct<32768, 1024>(ta, f1);
    BAR();
#pragma unroll
    for (int n = 0; n < 8; ++n) {
      sts128(65536u + (unsigned)wave * 16384u + (unsigned)lane * 16u, n * 1024, f0[n]);
      sts128(65536u + (unsigned)wave * 16384u + (unsigned)lane * 16u, 8192 + n * 1024, f1[n]);
    }
    BAR();
  }
  STAMP(1); FLUSH();
  const rsrc_t rres = mkrsrc(p.ws + OFF_X1 + layer * (2 << 20), 2048 * 1024);
  const rsrc_t rdst = layer == 0 ? mkrsrc(p.ws + OFF_X2, 2048 * 1024) : mkrsrc(p.out, 2048 * 1024);
  asm volatile(
  "v_mbcnt_lo_u32_b32 v0, -1, 0\n"
  "v_mbcnt_hi_u32_b32 v0, -1, v0\n"
  "v_and_b32 v1, 15, v0\n"
  "v_lshrrev_b32 v2, 4, v0\n"
  "v_lshlrev_b32 v120, 4, v0\n"
  "v_cmp_gt_u32 vcc, 14, v1\n"
  "v_lshlrev_b32 v121, 8, v1\n"
  "s_nop 0\n"
  "v_cndmask_b32_e64 v121, 0, v121, vcc\n"
  "v_lshlrev_b32 v6, 4, v2\n"
  "v_lshl_add_u32 v6, v1, 10, v6\n"
  "v_add_u32 v122, %[col], v6\n"
  "v_lshlrev_b32 v6, 3, v1\n"
  "v_mov_b32 v7, 0x7ff00000\n"
  "v_cmp_gt_u32 vcc, 16, v0\n"
  "s_nop 1\n"
  "v_cndmask_b32_e64 v123, v7, v6, vcc\n"
  "s_lshl_b32 s61, %[wave], 14\n"
  "s_add_u32 s61, s61, 65536\n"
  "v_add_u32 v124, s61, v120\n"
  "v_mov_b32 v125, 0x3f803f80\n"
  "s_lshl_b32 s73, %[wave], 12\n"
  "s_lshl_b32 s74, %[wave], 11\n"
  "s_add_u32 s74, s74, 49152\n"
  "s_and_b32 s65, s60, 1\n"
  "s_lshl_b32 s65, s65, 12\n"
  "s_add_u32 s65, s65, %[pin]\n"
  "s_add_u32 s66, s60, 1\n"
  "s_mul_i32 s70, s60, 57344\n"
  "s_add_u32 s70, s70, %[hbase]\n"
  "s_lshl_b32 s71, s60, 14\n"
  "s_lshl_b32 s72, s60, 11\n"
  "s_add_u32 s72, s72, %[stoff]\n"
  "s_add_u32 s75, s70, s73\n"
  "s_mov_b32 m0, s73\n"
  ".Ld_top_%=:\n"
  "s_mov_b32 s64, 0\n"
  ".Ld_poll_%=:\n"
  "buffer_load_dword v0, v121, %[rws], s65 offen sc1\n"
  "s_waitcnt vmcnt(0)\n"
  "v_cmp_gt_u32 vcc, s66, v0\n"
  "s_cbranch_vccnz .Ld_spin_%=\n"
  "buffer_load_dwordx4 v120, %[rws], s75 offen offset:0 sc1 lds\n"
  "buffer_load_dwordx4 v120, %[rws], s75 offen offset:1024 sc1 lds\n"
  "buffer_load_dwordx4 v120, %[rws], s75 offen offset:2048 sc1 lds\n"
  "buffer_load_dwordx4 v120, %[rws], s75 offen offset:3072 sc1 lds\n"
  "s_add_u32 m0, s73, 16384\n"
  "s_add_u32 s61, s75, 16384\n"
  "buffer_load_dwordx4 v120, %[rws], s61 offen offset:0 sc1 lds\n"
  "buffer_load_dwordx4 v120, %[rws], s61 offen offset:1024 sc1 lds\n"
  "buffer_load_dwordx4 v120, %[rws], s61 offen offset:2048 sc1 lds\n"
  "buffer_load_dwordx4 v120, %[rws], s61 offen offset:3072 sc1 lds\n"
  "s_add_u32 m0, s73, 32768\n"
  "s_add_u32 s61, s75, 32768\n"
  "buffer_load_dwordx4 v120, %[rws], s61 offen offset:0 sc1 lds\n"
  "buffer_load_dwordx4 v120, %[rws], s61 offen offset:1024 sc1 lds\n"
  "buffer_load_dwordx4 v120, %[rws], s61 offen offset:2048 sc1 lds\n"
  "buffer_load_dwordx4 v120, %[rws], s61 offen offset:3072 sc1 lds\n"
  "s_mov_b32 m0, s74\n"
  "s_add_u32 s61, s70, s74\n"
  "buffer_load_dwordx4 v120, %[rws], s61 offen offset:0 sc1 lds\n"
  "buffer_load_dwordx4 v120, %[rws], s61 offen offset:1024 sc1 lds\n"
  "buffer_load_dwordx4 v[40:43], v122, %[rres], s71 offen sc1\n"
  "s_waitcnt vmcnt(11)\n"
  "s_barrier\n"
  "ds_read_b128 v[0:3], v120 offset:0\n"
  "ds_read_b128 v[4:7], v120 offset:1024\n"
  "ds_read_b128 v[12:15], v120 offset:3072\n"
  "ds_read_b128 v[8:11], v120 offset:2048\n"
  "ds_read_b128 v[20:23], v120 offset:5120\n"
  "ds_read_b128 v[16:19], v120 offset:4096\n"
  "s_waitcnt lgkmcnt(5)\n"
  "v_mfma_f32_16x16x32_bf16 v[32:35], a[0:3], v[0:3], 0\n"
  "ds_read_b128 v[28:31], v120 offset:7168\n"
  "ds_read_b128 v[24:27], v120 offset:6144\n"
  "v_mfma_f32_16x16x32_bf16 v[36:39], a[4:7], v[0:3], 0\n"
  "s_waitcnt lgkmcnt(6)\n"
  "v_mfma_f32_16x16x32_bf16 v[32:35], a[8:11], v[4:7], v[32:35]\n"
  "ds_read_b128 v[48:51], v124 offset:0\n"
  "v_mfma_f32_16x16x32_bf16 v[36:39], a[12:15], v[4:7], v[36:39]\n"
  "s_waitcnt lgkmcnt(5)\n"
  "v_mfma_f32_16x16x32_bf16 v[32:35], a[16:19], v[8:11], v[32:35]\n"
  "ds_read_b128 v[4:7], v120 offset:9216\n"
  "ds_read_b128 v[0:3], v120 offset:8192\n"
  "v_mfma_f32_16x16x32_bf16 v[36:39], a[20:23], v[8:11], v[36:39]\n"
  "v_mfma_f32_16x16x32_bf16 v[32:35], a[24:27], v[12:15], v[32:35]\n"
  "ds_read_b128 v[52:55], v124 offset:1024\n"
  "v_mfma_f32_16x16x32_bf16 v[36:39], a[28:31], v[12:15], v[36:39]\n"
  "s_waitcnt lgkmcnt(6)\n"
  "v_mfma_f32_16x16x32_bf16 v[32:35], a[32:35], v[16:19], v[32:35]\n"
  "ds_read_b128 v[12:15], v120 offset:11264\n"
  "ds_read_b128 v[8:11], v120 offset:10240\n"
  "v_mfma_f32_16x16x32_bf16 v[36:39], a[36:39], v[16:19], v[36:39]\n"
  "v_mfma_f32_16x16x32_bf16 v[32:35], a[40:43], v[20:23], v[32:35]\n"
  "ds_read_b128 v[56:59], v124 offset:2048\n"
  "v_mfma_f32_16x16x32_bf16 v[36:39], a[44:47], v[20:23], v[36:39]\n"
  "s_waitcnt lgkmcnt(7)\n"
  "v_mfma_f32_16x16x32_bf16 v[32:35], a[48:51], v[24:27], v[32:35]\n"
  "ds_read_b128 v[20:23], v120 offset:13312\n"
  "ds_read_b128 v[16:19], v120 offset:12288\n"
  "v_mfma_f32_16x16x32_bf16 v[36:39], a[52:55], v[24:27], v[36:39]\n"
  "v_mfma_f32_16x16x32_bf16 v[32:35], a[56:59], v[28:31], v[32:35]\n"
  "ds_read_b128 v[60:63], v124 offset:3072\n"
  "v_mfma_f32_16x16x32_bf16 v[36:39], a[60:63], v[28:31], v[36:39]\n"
  "s_waitcnt lgkmcnt(7)\n"
  "v_mfma_f32_16x16x32_bf16 v[32:35], a[64:67], v[0:3], v[32:35]\n"
  "ds_read_b128 v[28:31], v120 offset:15360\n"
  "ds_read_b128 v[24:27], v120 offset:14336\n"
  "v_mfma_f32_16x16x32_bf16 v[36:39], a[68:71], v[0:3], v[36:39]\n"
  "v_mfma_f32_16x16x32_bf16 v[32:35], a[72:75], v[4:7], v[32:35]\n"
  "ds_read_b128 v[64:67], v124 offset:4096\n"
  "v_mfma_f32_16x16x32_bf16 v[36:39], a[76:79], v[4:7], v[36:39]\n"
  "s_waitcnt vmcnt(7)\n"
  "s_barrier\n"
  "s_waitcnt lgkmcnt(7)\n"
  "v_mfma_f32_16x16x32_bf16 v[32:35], a[80:83], v[8:11], v[32:35]\n"
  "ds_read_b128 v[4:7], v120 offset:17408\n"
  "ds_read_b128 v[0:3], v120 offset:16384\n"
  "v_mfma_f32_16x16x32_bf16 v[36:39], a[84:87], v[8:11], v[36:39]\n"
  "v_mfma_f32_16x16x32_bf16 v[32:35], a[88:91], v[12:15], v[32:35]\n"
  "ds_read_b128 v[68:71], v124 offset:5120\n"
  "v_mfma_f32_16x16x32_bf16 v[36:39], a[92:95], v[12:15], v[36:39]\n"
  "s_waitcnt lgkmcnt(7)\n"
  "v_mfma_f32_16x16x32_bf16 v[32:35], a[96:99], v[16:19], v[32:35]\n"
  "ds_read_b128 v[12:15], v120 offset:19456\n"
  "ds_read_b128 v[8:11], v120 offset:18432\n"
  "v_mfma_f32_16x16x32_bf16 v[36:39], a[100:103], v[16:19], v[36:39]\n"
  "v_mfma_f32_16x16x32_bf16 v[32:35], a[104:107], v[20:23], v[32:35]\n"
  "ds_read_b128 v[72:75], v124 offset:6144\n"
  "v_mfma_f32_16x16x32_bf16 v[36:39], a[108:111], v[20:23], v[36:39]\n"
  "s_waitcnt lgkmcnt(7)\n"
  "v_mfma_f32_16x16x32_bf16 v[32:35], a[112:115], v[24:27], v[32:35]\n"
  "ds_read_b128 v[20:23], v120 offset:21504\n"
  "ds_read_b128 v[16:19], v120 offset:20480\n"
  "v_mfma_f32_16x16x32_bf16 v[36:39], a[116:119], v[24:27], v[36:39]\n"
  "v_mfma_f32_16x16x32_bf16 v[32:35], a[120:123], v[28:31], v[32:35]\n"
  "ds_read_b128 v[76:79], v124 offset:7168\n"
  "v_mfma_f32_16x16x32_bf16 v[36:39], a[124:127], v[28:31], v[36:39]\n"
  "s_waitcnt lgkmcnt(7)\n"
  "v_mfma_f32_16x16x32_bf16 v[32:35], a[128:131], v[0:3], v[32:35]\n"
  "ds_read_b128 v[28:31], v120 offset:23552\n"
  "ds_read_b128 v[24:27], v120 offset:22528\n"
  "v_mfma_f32_16x16x32_bf16 v[36:39], a[132:135], v[0:3], v[36:39]\n"
  "v_mfma_f32_16x16x32_bf16 v[32:35], a[136:139], v[4:7], v[32:35]\n"
  "ds_read_b128 v[80:83], v124 offset:8192\n"
  "v_mfma_f32_16x16x32_bf16 v[36:39], a[140:143], v[4:7], v[36:39]\n"
  "s_waitcnt lgkmcnt(7)\n"
  "v_mfma_f32_16x16x32_bf16 v[32:35], a[144:147], v[8:11], v[32:35]\n"
  "ds_read_b128 v[4:7], v120 offset:25600\n"
  "ds_read_b128 v[0:3], v120 offset:24576\n"
  "v_mfma_f32_16x16x32_bf16 v[36:39], a[148:151], v[8:11], v[36:39]\n"
  "v_mfma_f32_16x16x32_bf16 v[32:35], a[152:155], v[12:15], v[32:35]\n"
  "ds_read_b128 v[84:87], v124 offset:9216\n"
  "v_mfma_f32_16x16x32_bf16 v[36:39], a[156:159], v[12:15], v[36:39]\n"
  "s_waitcnt lgkmcnt(7)\n"
  "v_mfma_f32_16x16x32_bf16 v[32:35], a[160:163], v[16:19], v[32:35]\n"
  "ds_read_b128 v[12:15], v120 offset:27648\n"
  "ds_read_b128 v[8:11], v120 offset:26624\n"
  "v_mfma_f32_16x16x32_bf16 v[36:39], a[164:167], v[16:19], v[36:39]\n"
  "v_mfma_f32_16x16x32_bf16 v[32:35], a[168:171], v[20:23], v[32:35]\n"
  "ds_read_b128 v[88:91], v124 offset:10240\n"
  "v_mfma_f32_16x16x32_bf16 v[36:39], a[172:175], v[20:23], v[36:39]\n"
  "s_waitcnt lgkmcnt(7)\n"
  "v_mfma_f32_16x16x32_bf16 v[32:35], a[176:179], v[24:27], v[32:35]\n"
  "ds_read_b128 v[20:23], v120 offset:29696\n"
  "ds_read_b128 v[16:19], v120 offset:28672\n"
  "v_mfma_f32_16x16x32_bf16 v[36:39], a[180:183], v[24:27], v[36:39]\n"
  "v_mfma_f32_16x16x32_bf16 v[32:35], a[184:187], v[28:31], v[32:35]\n"
  "ds_read_b128 v[92:95], v124 offset:11264\n"
  "v_mfma_f32_16x16x32_bf16 v[36:39], a[188:191], v[28:31], v[36:39]\n"
  "s_waitcnt lgkmcnt(7)\n"
  "v_mfma_f32_16x16x32_bf16 v[32:35], a[192:195], v[0:3], v[32:35]\n"
  "ds_read_b128 v[28:31], v120 offset:31744\n"
  "ds_read_b128 v[24:27], v120 offset:30720\n"
  "v_mfma_f32_16x16x32_bf16 v[36:39], a[196:199], v[0:3], v[36:39]\n"
  "v_mfma_f32_16x16x32_bf16 v[32:35], a[200:203], v[4:7], v[32:35]\n"
  "ds_read_b128 v[96:99], v124 offset:12288\n"
  "v_mfma_f32_16x16x32_bf16 v[36:39], a[204:207], v[4:7], v[36:39]\n"
  "s_waitcnt vmcnt(3)\n"
  "s_barrier\n"
  "s_waitcnt lgkmcnt(7)\n"
  "v_mfma_f32_16x16x32_bf16 v[32:35], a[208:211], v[8:11], v[32:35]\n"
  "ds_read_b128 v[4:7], v120 offset:33792\n"
  "ds_read_b128 v[0:3], v120 offset:32768\n"
  "v_mfma_f32_16x16x32_bf16 v[36:39], a[212:215], v[8:11], v[36:39]\n"
  "v_mfma_f32_16x16x32_bf16 v[32:35], a[216:219], v[12:15], v[32:35]\n"
  "ds_read_b128 v[100:103], v124 offset:13312\n"
  "v_mfma_f32_16x16x32_bf16 v[36:39], a[220:223], v[12:15], v[36:39]\n"
  "s_waitcnt lgkmcnt(7)\n"
  "v_mfma_f32_16x16x32_bf16 v[32:35], a[224:227], v[16:19], v[32:35]\n"
  "ds_read_b128 v[12:15], v120 offset:35840\n"
  "ds_read_b128 v[8:11], v120 offset:34816\n"
  "v_mfma_f32_16x16x32_bf16 v[36:39], a[228:231], v[16:19], v[36:39]\n"
  "v_mfma_f32_16x16x32_bf16 v[32:35], a[232:235], v[20:23], v[32:35]\n"
  "ds_read_b128 v[104:107], v124 offset:14336\n"
  "v_mfma_f32_16x16x32_bf16 v[36:39], a[236:239], v[20:23], v[36:39]\n"
  "s_waitcnt lgkmcnt(7)\n"
  "v_mfma_f32_16x16x32_bf16 v[32:35], a[240:243], v[24:27], v[32:35]\n"
  "ds_read_b128 v[20:23], v120 offset:37888\n"
  "ds_read_b128 v[16:19], v120 offset:36864\n"
  "v_mfma_f32_16x16x32_bf16 v[36:39], a[244:247], v[24:27], v[36:39]\n"
  "v_mfma_f32_16x16x32_bf16 v[32:35], a[248:251], v[28:31], v[32:35]\n"
  "ds_read_b128 v[108:111], v124 offset:15360\n"
  "v_mfma_f32_16x16x32_bf16 v[36:39], a[252:255], v[28:31], v[36:39]\n"
  "s_waitcnt lgkmcnt(7)\n"
  "v_mfma_f32_16x16x32_bf16 v[32:35], v[128:131], v[0:3], v[32:35]\n"
  "ds_read_b128 v[28:31], v120 offset:39936\n"
  "ds_read_b128 v[24:27], v120 offset:38912\n"
  "v_mfma_f32_16x16x32_bf16 v[36:39], v[132:135], v[0:3], v[36:39]\n"
  "v_mfma_f32_16x16x32_bf16 v[32:35], v[136:139], v[4:7], v[32:35]\n"
  "v_mfma_f32_16x16x32_bf16 v[36:39], v[140:143], v[4:7], v[36:39]\n"
  "s_waitcnt lgkmcnt(6)\n"
  "v_mfma_f32_16x16x32_bf16 v[32:35], v[144:147], v[8:11], v[32:35]\n"
  "ds_read_b128 v[4:7], v120 offset:41984\n"
  "ds_read_b128 v[0:3], v120 offset:40960\n"
  "v_mfma_f32_16x16x32_bf16 v[36:39], v[148:151], v[8:11], v[36:39]\n"
  "v_mfma_f32_16x16x32_bf16 v[32:35], v[152:155], v[12:15], v[32:35]\n"
  "v_mfma_f32_16x16x32_bf16 v[36:39], v[156:159], v[12:15], v[36:39]\n"
  "s_waitcnt lgkmcnt(5)\n"
  "v_mfma_f32_16x16x32_bf16 v[32:35], v[160:163], v[16:19], v[32:35]\n"
  "ds_read_b128 v[12:15], v120 offset:44032\n"
  "ds_read_b128 v[8:11], v120 offset:43008\n"
  "v_mfma_f32_16x16x32_bf16 v[36:39], v[164:167], v[16:19], v[36:39]\n"
  "v_mfma_f32_16x16x32_bf16 v[32:35], v[168:171], v[20:23], v[32:35]\n"
  "v_mfma_f32_16x16x32_bf16 v[36:39], v[172:175], v[20:23], v[36:39]\n"
  "s_waitcnt lgkmcnt(4)\n"
  "v_mfma_f32_16x16x32_bf16 v[32:35], v[176:179], v[24:27], v[32:35]\n"
  "ds_read_b128 v[20:23], v120 offset:46080\n"
  "ds_read_b128 v[16:19], v120 offset:45056\n"
  "v_mfma_f32_16x16x32_bf16 v[36:39], v[180:183], v[24:27], v[36:39]\n"
  "v_mfma_f32_16x16x32_bf16 v[32:35], v[184:187], v[28:31], v[32:35]\n"
  "v_mfma_f32_16x16x32_bf16 v[36:39], v[188:191], v[28:31], v[36:39]\n"
  "s_waitcnt lgkmcnt(4)\n"
  "v_mfma_f32_16x16x32_bf16 v[32:35], v[192:195], v[0:3], v[32:35]\n"
  "ds_read_b128 v[28:31], v120 offset:48128\n"
  "ds_read_b128 v[24:27], v120 offset:47104\n"
  "v_mfma_f32_16x16x32_bf16 v[36:39], v[196:199], v[0:3], v[36:39]\n"
  "v_mfma_f32_16x16x32_bf16 v[32:35], v[200:203], v[4:7], v[32:35]\n"
  "v_mfma_f32_16x16x32_bf16 v[36:39], v[204:207], v[4:7], v[36:39]\n"
  "s_waitcnt vmcnt(1)\n"
  "s_barrier\n"
  "s_waitcnt lgkmcnt(4)\n"
  "v_mfma_f32_16x16x32_bf16 v[32:35], v[208:211], v[8:11], v[32:35]\n"
  "ds_read_b128 v[4:7], v120 offset:50176\n"
  "ds_read_b128 v[0:3], v120 offset:49152\n"
  "v_mfma_f32_16x16x32_bf16 v[36:39], v[212:215], v[8:11], v[36:39]\n"
  "v_mfma_f32_16x16x32_bf16 v[32:35], v[216:219], v[12:15], v[32:35]\n"
  "v_mfma_f32_16x16x32_bf16 v[36:39], v[220:223], v[12:15], v[36:39]\n"
  "s_waitcnt lgkmcnt(4)\n"
  "v_mfma_f32_16x16x32_bf16 v[32:35], v[224:227], v[16:19], v[32:35]\n"
  "ds_read_b128 v[12:15], v120 offset:52224\n"
  "ds_read_b128 v[8:11], v120 offset:51200\n"
  "v_mfma_f32_16x16x32_bf16 v[36:39], v[228:231], v[16:19], v[36:39]\n"
  "v_mfma_f32_16x16x32_bf16 v[32:35], v[232:235], v[20:23], v[32:35]\n"
  "v_mfma_f32_16x16x32_bf16 v[36:39], v[236:239], v[20:23], v[36:39]\n"
  "s_waitcnt lgkmcnt(4)\n"
  "v_mfma_f32_16x16x32_bf16 v[32:35], v[240:243], v[24:27], v[32:35]\n"
  "ds_read_b128 v[20:23], v120 offset:54272\n"
  "ds_read_b128 v[16:19], v120 offset:53248\n"
  "v_mfma_f32_16x16x32_bf16 v[36:39], v[244:247], v[24:27], v[36:39]\n"
  "v_mfma_f32_16x16x32_bf16 v[32:35], v[248:251], v[28:31], v[32:35]\n"
  "v_mfma_f32_16x16x32_bf16 v[36:39], v[252:255], v[28:31], v[36:39]\n"
  "s_waitcnt lgkmcnt(4)\n"
  "v_mfma_f32_16x16x32_bf16 v[32:35], v[48:51], v[0:3], v[32:35]\n"
  "ds_read_b128 v[28:31], v120 offset:56320\n"
  "ds_read_b128 v[24:27], v120 offset:55296\n"
  "v_mfma_f32_16x16x32_bf16 v[36:39], v[52:55], v[0:3], v[36:39]\n"
  "v_mfma_f32_16x16x32_bf16 v[32:35], v[56:59], v[4:7], v[32:35]\n"
  "v_mfma_f32_16x16x32_bf16 v[36:39], v[60:63], v[4:7], v[36:39]\n"
  "s_waitcnt lgkmcnt(4)\n"
  "v_mfma_f32_16x16x32_bf16 v[32:35], v[64:67], v[8:11], v[32:35]\n"
  "v_mfma_f32_16x16x32_bf16 v[36:39], v[68:71], v[8:11], v[36:39]\n"
  "v_mfma_f32_16x16x32_bf16 v[32:35], v[72:75], v[12:15], v[32:35]\n"
  "v_mfma_f32_16x16x32_bf16 v[36:39], v[76:79], v[12:15], v[36:39]\n"
  "s_waitcnt lgkmcnt(2)\n"
  "v_mfma_f32_16x16x32_bf16 v[32:35], v[80:83], v[16:19], v[32:35]\n"
  "v_mfma_f32_16x16x32_bf16 v[36:39], v[84:87], v[16:19], v[36:39]\n"
  "v_mfma_f32_16x16x32_bf16 v[32:35], v[88:91], v[20:23], v[32:35]\n"
  "v_mfma_f32_16x16x32_bf16 v[36:39], v[92:95], v[20:23], v[36:39]\n"
  "s_waitcnt lgkmcnt(0)\n"
  "v_mfma_f32_16x16x32_bf16 v[32:35], v[96:99], v[24:27], v[32:35]\n"
  "v_mfma_f32_16x16x32_bf16 v[36:39], v[100:103], v[24:27], v[36:39]\n"
  "v_mfma_f32_16x16x32_bf16 v[32:35], v[104:107], v[28:31], v[32:35]\n"
  "v_mfma_f32_16x16x32_bf16 v[36:39], v[108:111], v[28:31], v[36:39]\n"
  "s_nop 4\n"
  "v_cvt_pk_bf16_f32 v32, v32, v33\n"
  "v_cvt_pk_bf16_f32 v33, v34, v35\n"
  "s_nop 1\n"
  "v_cvt_pk_bf16_f32 v34, v36, v37\n"
  "v_cvt_pk_bf16_f32 v35, v38, v39\n"
  "v_lshlrev_b32 v4, 16, v32\n"
  "v_and_b32 v5, 0xffff0000, v32\n"
  "s_waitcnt vmcnt(0)\n"
  "v_lshlrev_b32 v6, 16, v40\n"
  "v_and_b32 v7, 0xffff0000, v40\n"
  "v_pk_add_f32 v[4:5], v[4:5], v[6:7]\n"
  "s_nop 0\n"
  "v_cvt_pk_bf16_f32 v0, v4, v5\n"
  "v_lshlrev_b32 v4, 16, v33\n"
  "v_and_b32 v5, 0xffff0000, v33\n"
  "v_lshlrev_b32 v6, 16, v41\n"
  "v_and_b32 v7, 0xffff0000, v41\n"
  "v_pk_add_f32 v[4:5], v[4:5], v[6:7]\n"
  "s_nop 0\n"
  "v_cvt_pk_bf16_f32 v1, v4, v5\n"
  "v_lshlrev_b32 v4, 16, v34\n"
  "v_and_b32 v5, 0xffff0000, v34\n"
  "v_lshlrev_b32 v6, 16, v42\n"
  "v_and_b32 v7, 0xffff0000, v42\n"
  "v_pk_add_f32 v[4:5], v[4:5], v[6:7]\n"
  "s_nop 0\n"
  "v_cvt_pk_bf16_f32 v2, v4, v5\n"
  "v_lshlrev_b32 v4, 16, v35\n"
  "v_and_b32 v5, 0xffff0000, v35\n"
  "v_lshlrev_b32 v6, 16, v43\n"
  "v_and_b32 v7, 0xffff0000, v43\n"
  "v_pk_add_f32 v[4:5], v[4:5], v[6:7]\n"
  "s_nop 0\n"
  "v_cvt_pk_bf16_f32 v3, v4, v5\n"
  "buffer_store_dwordx4 v[0:3], v122, %[rdst], s71 offen sc1\n"
  "v_dot2_f32_bf16 v8, v0, v125, 0\n"
  "v_dot2_f32_bf16 v9, v0, v0, 0\n"
  "v_dot2_f32_bf16 v8, v1, v125, v8\n"
  "v_dot2_f32_bf16 v9, v1, v1, v9\n"
  "v_dot2_f32_bf16 v8, v2, v125, v8\n"
  "v_dot2_f32_bf16 v9, v2, v2, v9\n"
  "v_dot2_f32_bf16 v8, v3, v125, v8\n"
  "v_dot2_f32_bf16 v9, v3, v3, v9\n"
  "s_nop 2\n"
  "s_nop 0\n"
  "v_permlane16_swap_b32 v8, v9\n"
  "v_add_f32 v8, v8, v9\n"
  "v_mov_b32 v9, v8\n"
  "s_nop 1\n"
  "v_permlane32_swap_b32 v8, v9\n"
  "v_add_f32 v8, v8, v9\n"
  "v_mov_b32 v9, v8\n"
  "s_nop 1\n"
  "v_permlane16_swap_b32 v8, v9\n"
  "buffer_store_dwordx2 v[8:9], v123, %[rws], s72 offen sc1\n"
  "s_lshr_b32 s62, s60, 1\n"
  "s_add_u32 s62, s62, 1\n"
  "v_mov_b32 v0, s62\n"
  "s_mov_b32 s63, s60\n"
  "s_add_u32 s60, s60, 3\n"
  "s_and_b32 s65, s60, 1\n"
  "s_lshl_b32 s65, s65, 12\n"
  "s_add_u32 s65, s65, %[pin]\n"
  "s_add_u32 s66, s60, 1\n"
  "s_mul_i32 s70, s60, 57344\n"
  "s_add_u32 s70, s70, %[hbase]\n"
  "s_lshl_b32 s71, s60, 14\n"
  "s_lshl_b32 s72, s60, 11\n"
  "s_add_u32 s72, s72, %[stoff]\n"
  "s_add_u32 s75, s70, s73\n"
  "s_mov_b32 m0, s73\n"
  "s_waitcnt vmcnt(0)\n"
  "s_barrier\n"
  "buffer_store_dword v0, off, %[rws], %[pout] sc1\n"
  "s_cmp_lt_u32 s60, 128\n"
  "s_cbranch_scc1 .Ld_top_%=\n"
  "s_endpgm\n"
  ".Ld_spin_%=:\n"
  "s_add_u32 s64, s64, 1\n"
  "s_sleep 1\n"
  "s_cmp_lt_u32 s64, 262144\n"
  "s_cbranch_scc1 .Ld_poll_%=\n"
  "v_mov_b32 v0, 1\n"
  "buffer_store_dword v0, off, %[rws], 0 offset:2048 sc1\n"
  "s_endpgm\n"
      :
      : [rws] "s"(rws), [rres] "s"(rres), [rdst] "s"(rdst), "{s60}"(rep), [wave] "s"(wave), [col] "s"(cb * 256 + wave * 64),
        [pin] "s"(OFF_PG + layer * 32 * PST), [pout] "s"(OFF_PD + (rep * 4 + cb) * PST + (wave || layer ? 128 : 0)),
        [hbase] "s"(OFF_HB + layer * NUNIT * 57344), [stoff] "s"(OFF_ST2 + (layer + 1) * 262144 + (cb * 4 + wave) * 128),
        [prof] "s"(OFF_PROF + (((role * 4 + wave) * 129) << 7))
      : "memory", "vcc", "scc", VCLOB, SCLOB);
  __builtin_unreachable();
}

DI void role_G(const Params& p, const int role, const int wave, const int layer, const int rep, const int gi) {
  const int lane = lane_id(), g = lane >> 4;
  const rsrc_t rws = mkrsrc(p.ws, WS_USED);
  int pidx = 128; (void)pidx;
  unsigned pv = 0; (void)pv;
  STAMPR(); STAMP(0);
  constexpr int SG = 0, SU = 32;
  f32x4 cs[4];
  auto src_of = [&](int c) -> const char* { return (const char*)(c < 4 ? p.wg : p.wu) + ((size_t)(128 * (c & 3)) * FF + gi * 128) * 2; };
  const int q = (lane >> 2) & 3, pp = lane & 3;
  const unsigned trb = (unsigned)(8 * g + q) * 256u + (unsigned)(((4 * wave + pp) ^ (q << 2)) << 4);
#pragma unroll
  for (int c = 0; c < 4; ++c) chunk_issue(src_of(c), FF * 2, (unsigned)c * 32768u, wave, lane);
  v4u ones = {ONES, ONES, ONES, ONES};
  asm volatile("s_nop 1" : "+v"(ones));
  sfor<8>([&](auto C) {
    constexpr int c = C;
    wait_vm<(c <= 4 ? 24 : 8 * (7 - c))>();
    BAR();
    if (c + 4 < 8) chunk_issue(src_of(c + 4), FF * 2, (unsigned)((c + 4) % 5) * 32768u, wave, lane);
    tr_quad<(c < 4 ? SG : SU), 4 * (c & 3)>(trb + (unsigned)(c % 5) * 32768u);
    sfor<8>([&](auto I) {
      constexpr int kk = 4 * (c & 3) + (I >> 1), j = I & 1;
      mfWA<(c < 4 ? SG : SU) + 16 * j + kk, kk == 0>(cs[(c < 4 ? 0 : 2) + j], ones);
    });
  });
  asm volatile("s_nop 7" : "+v"(cs[0]), "+v"(cs[1]), "+v"(cs[2]), "+v"(cs[3]));
  BAR();
  STAMP(1); FLUSH();
  const rsrc_t rx = mkrsrc(p.ws + OFF_X1 + layer * (2 << 20), 2048 * 1024);
  asm volatile(
  "v_mbcnt_lo_u32_b32 v0, -1, 0\n"
  "v_mbcnt_hi_u32_b32 v0, -1, v0\n"
  "v_and_b32 v1, 15, v0\n"
  "v_lshrrev_b32 v2, 4, v0\n"
  "v_lshlrev_b32 v120, 4, v0\n"
  "v_cmp_gt_u32 vcc, 8, v1\n"
  "v_lshlrev_b32 v121, 8, v1\n"
  "s_nop 0\n"
  "v_cndmask_b32_e64 v121, 0, v121, vcc\n"
  "s_lshl_b32 s61, %[wave], 6\n"
  "v_xor_b32 v112, s61, v120\n"
  "v_xor_b32 v113, 16, v112\n"
  "v_xor_b32 v114, 32, v112\n"
  "v_xor_b32 v115, 48, v112\n"
  "v_xor_b32 v6, v2, v1\n"
  "v_and_b32 v6, 3, v6\n"
  "v_and_or_b32 v6, v1, 12, v6\n"
  "v_lshlrev_b32 v6, 4, v6\n"
  "v_lshl_add_u32 v116, v1, 10, v6\n"
  "v_xor_b32 v117, 64, v116\n"
  "v_xor_b32 v118, 128, v116\n"
  "v_xor_b32 v119, 192, v116\n"
  "v_lshlrev_b32 v6, 4, v1\n"
  "v_lshl_add_u32 v122, v2, 9, v6\n"
  "s_lshl_b32 m0, %[wave], 12\n"
  "s_mov_b32 s67, s60\n"
  "s_lshl_b32 s65, s67, 11\n"
  "s_add_u32 s65, s65, %[pin]\n"
  "s_add_u32 s66, s60, 1\n"
  "s_lshl_b32 s70, s60, 14\n"
  "s_add_u32 s70, s70, m0\n"
  "s_lshl_b32 s71, s60, 11\n"
  "s_add_u32 s71, s71, %[stoff]\n"
  "s_mul_i32 s72, s60, 57344\n"
  "s_add_u32 s72, s72, %[hbase]\n"
  ".Lg_top_%=:\n"
  "s_mov_b32 s64, 0\n"
  ".Lg_poll_%=:\n"
  "buffer_load_dword v0, v121, %[rws], s65 offen sc1\n"
  "s_waitcnt vmcnt(0)\n"
  "v_cmp_gt_u32 vcc, s66, v0\n"
  "s_cbranch_vccnz .Lg_spin_%=\n"
  "buffer_load_dwordx4 v112, %[rx], s70 offen offset:0 sc1 lds\n"
  "buffer_load_dwordx4 v113, %[rx], s70 offen offset:1024 sc1 lds\n"
  "buffer_load_dwordx4 v114, %[rx], s70 offen offset:2048 sc1 lds\n"
  "buffer_load_dwordx4 v115, %[rx], s70 offen offset:3072 sc1 lds\n"
  "buffer_load_dwordx2 v[64:65], v122, %[rws], s71 offen sc1\n"
  "buffer_load_dwordx2 v[66:67], v122, %[rws], s71 offen offset:256 sc1\n"
  "s_waitcnt vmcnt(2)\n"
  "s_barrier\n"
  "ds_read_b128 v[0:3], v116 offset:0\n"
  "ds_read_b128 v[4:7], v117 offset:0\n"
  "ds_read_b128 v[12:15], v119 offset:0\n"
  "ds_read_b128 v[8:11], v118 offset:0\n"
  "s_waitcnt lgkmcnt(3)\n"
  "v_mfma_f32_16x16x32_bf16 v[68:71], a[0:3], v[0:3], 0\n"
  "ds_read_b128 v[20:23], v117 offset:256\n"
  "ds_read_b128 v[16:19], v116 offset:256\n"
  "v_mfma_f32_16x16x32_bf16 v[76:79], a[128:131], v[0:3], 0\n"
  "s_waitcnt lgkmcnt(4)\n"
  "v_mfma_f32_16x16x32_bf16 v[68:71], a[4:7], v[4:7], v[68:71]\n"
  "ds_read_b128 v[28:31], v119 offset:256\n"
  "ds_read_b128 v[24:27], v118 offset:256\n"
  "v_mfma_f32_16x16x32_bf16 v[76:79], a[132:135], v[4:7], v[76:79]\n"
  "s_waitcnt lgkmcnt(4)\n"
  "v_mfma_f32_16x16x32_bf16 v[68:71], a[8:11], v[8:11], v[68:71]\n"
  "ds_read_b128 v[36:39], v117 offset:512\n"
  "ds_read_b128 v[32:35], v116 offset:512\n"
  "v_mfma_f32_16x16x32_bf16 v[76:79], a[136:139], v[8:11], v[76:79]\n"
  "v_mfma_f32_16x16x32_bf16 v[68:71], a[12:15], v[12:15], v[68:71]\n"
  "ds_read_b128 v[44:47], v119 offset:512\n"
  "ds_read_b128 v[40:43], v118 offset:512\n"
  "v_mfma_f32_16x16x32_bf16 v[76:79], a[140:143], v[12:15], v[76:79]\n"
  "s_waitcnt lgkmcnt(6)\n"
  "v_mfma_f32_16x16x32_bf16 v[68:71], a[16:19], v[16:19], v[68:71]\n"
  "ds_read_b128 v[52:55], v117 offset:768\n"
  "ds_read_b128 v[48:51], v116 offset:768\n"
  "v_mfma_f32_16x16x32_bf16 v[76:79], a[144:147], v[16:19], v[76:79]\n"
  "v_mfma_f32_16x16x32_bf16 v[68:71], a[20:23], v[20:23], v[68:71]\n"
  "ds_read_b128 v[60:63], v119 offset:768\n"
  "ds_read_b128 v[56:59], v118 offset:768\n"
  "v_mfma_f32_16x16x32_bf16 v[76:79], a[148:151], v[20:23], v[76:79]\n"
  "s_waitcnt lgkmcnt(8)\n"
  "v_mfma_f32_16x16x32_bf16 v[68:71], a[24:27], v[24:27], v[68:71]\n"
  "s_waitcnt vmcnt(0)\n"
  "v_add_f32 v64, v64, v66\n"
  "v_mfma_f32_16x16x32_bf16 v[76:79], a[152:155], v[24:27], v[76:79]\n"
  "v_add_f32 v65, v65, v67\n"
  "v_mfma_f32_16x16x32_bf16 v[68:71], a[28:31], v[28:31], v[68:71]\n"
  "v_permlane16_swap_b32 v64, v65\n"
  "v_add_f32 v64, v64, v65\n"
  "v_mfma_f32_16x16x32_bf16 v[76:79], a[156:159], v[28:31], v[76:79]\n"
  "v_mov_b32 v65, v64\n"
  "s_waitcnt lgkmcnt(6)\n"
  "v_mfma_f32_16x16x32_bf16 v[68:71], a[32:35], v[32:35], v[68:71]\n"
  "v_mfma_f32_16x16x32_bf16 v[76:79], a[160:163], v[32:35], v[76:79]\n"
  "v_permlane32_swap_b32 v64, v65\n"
  "v_add_f32 v64, v64, v65\n"
  "v_mfma_f32_16x16x32_bf16 v[68:71], a[36:39], v[36:39], v[68:71]\n"
  "v_mov_b32 v65, v64\n"
  "v_mfma_f32_16x16x32_bf16 v[76:79], a[164:167], v[36:39], v[76:79]\n"
  "v_permlane16_swap_b32 v64, v65\n"
  "s_waitcnt lgkmcnt(4)\n"
  "v_mfma_f32_16x16x32_bf16 v[68:71], a[40:43], v[40:43], v[68:71]\n"
  "v_mul_f32 v66, 0x3b000000, v64\n"
  "v_mul_f32 v65, 0x3b000000, v65\n"
  "v_mfma_f32_16x16x32_bf16 v[76:79], a[168:171], v[40:43], v[76:79]\n"
  "v_fma_f32 v65, -v66, v66, v65\n"
  "v_max_f32 v65, 0, v65\n"
  "v_mfma_f32_16x16x32_bf16 v[68:71], a[44:47], v[44:47], v[68:71]\n"
  "v_add_f32 v65, 0x3727c5ac, v65\n"
  "v_mfma_f32_16x16x32_bf16 v[76:79], a[172:175], v[44:47], v[76:79]\n"
  "s_waitcnt lgkmcnt(2)\n"
  "v_mfma_f32_16x16x32_bf16 v[68:71], a[48:51], v[48:51], v[68:71]\n"
  "v_rsq_f32 v64, v65\n"
  "v_mfma_f32_16x16x32_bf16 v[76:79], a[176:179], v[48:51], v[76:79]\n"
  "v_mfma_f32_16x16x32_bf16 v[68:71], a[52:55], v[52:55], v[68:71]\n"
  "v_mul_f32 v66, -v64, v66\n"
  "v_mul_f32 v84, v66, v96\n"
  "v_mfma_f32_16x16x32_bf16 v[76:79], a[180:183], v[52:55], v[76:79]\n"
  "v_mul_f32 v85, v66, v97\n"
  "s_waitcnt lgkmcnt(0)\n"
  "v_mfma_f32_16x16x32_bf16 v[68:71], a[56:59], v[56:59], v[68:71]\n"
  "v_mul_f32 v86, v66, v98\n"
  "v_mul_f32 v87, v66, v99\n"
  "v_mfma_f32_16x16x32_bf16 v[76:79], a[184:187], v[56:59], v[76:79]\n"
  "v_mul_f32 v88, v66, v104\n"
  "v_mul_f32 v89, v66, v105\n"
  "v_mfma_f32_16x16x32_bf16 v[68:71], a[60:63], v[60:63], v[68:71]\n"
  "v_mul_f32 v90, v66, v106\n"
  "v_mul_f32 v91, v66, v107\n"
  "v_mfma_f32_16x16x32_bf16 v[76:79], a[188:191], v[60:63], v[76:79]\n"
  "v_mfma_f32_16x16x32_bf16 v[72:75], a[64:67], v[0:3], 0\n"
  "v_mfma_f32_16x16x32_bf16 v[80:83], a[192:195], v[0:3], 0\n"
  "v_fmac_f32 v84, v64, v68\n"
  "v_fmac_f32 v85, v64, v69\n"
  "v_mfma_f32_16x16x32_bf16 v[72:75], a[68:71], v[4:7], v[72:75]\n"
  "v_fmac_f32 v86, v64, v70\n"
  "v_fmac_f32 v87, v64, v71\n"
  "v_mfma_f32_16x16x32_bf16 v[80:83], a[196:199], v[4:7], v[80:83]\n"
  "v_fmac_f32 v88, v64, v76\n"
  "v_fmac_f32 v89, v64, v77\n"
  "v_mfma_f32_16x16x32_bf16 v[72:75], a[72:75], v[8:11], v[72:75]\n"
  "v_fmac_f32 v90, v64, v78\n"
  "v_fmac_f32 v91, v64, v79\n"
  "v_mfma_f32_16x16x32_bf16 v[80:83], a[200:203], v[8:11], v[80:83]\n"
  "v_cvt_pk_bf16_f32 v84, v84, v85\n"
  "v_cvt_pk_bf16_f32 v86, v86, v87\n"
  "v_mfma_f32_16x16x32_bf16 v[72:75], a[76:79], v[12:15], v[72:75]\n"
  "v_cvt_pk_bf16_f32 v88, v88, v89\n"
  "v_cvt_pk_bf16_f32 v90, v90, v91\n"
  "v_mfma_f32_16x16x32_bf16 v[80:83], a[204:207], v[12:15], v[80:83]\n"
  "v_and_b32 v85, 0xffff0000, v84\n"
  "v_lshlrev_b32 v84, 16, v84\n"
  "v_mfma_f32_16x16x32_bf16 v[72:75], a[80:83], v[16:19], v[72:75]\n"
  "v_and_b32 v89, 0xffff0000, v88\n"
  "v_lshlrev_b32 v88, 16, v88\n"
  "v_mfma_f32_16x16x32_bf16 v[80:83], a[208:211], v[16:19], v[80:83]\n"
  "v_max_f32 v84, 0, v84\n"
  "v_max_f32 v85, 0, v85\n"
  "v_mfma_f32_16x16x32_bf16 v[72:75], a[84:87], v[20:23], v[72:75]\n"
  "v_mul_f32 v84, v84, v88\n"
  "v_mul_f32 v85, v85, v89\n"
  "v_mfma_f32_16x16x32_bf16 v[80:83], a[212:215], v[20:23], v[80:83]\n"
  "v_cvt_pk_bf16_f32 v92, v84, v85\n"
  "v_and_b32 v87, 0xffff0000, v86\n"
  "v_mfma_f32_16x16x32_bf16 v[72:75], a[88:91], v[24:27], v[72:75]\n"
  "v_lshlrev_b32 v86, 16, v86\n"
  "v_and_b32 v91, 0xffff0000, v90\n"
  "v_mfma_f32_16x16x32_bf16 v[80:83], a[216:219], v[24:27], v[80:83]\n"
  "v_lshlrev_b32 v90, 16, v90\n"
  "v_max_f32 v86, 0, v86\n"
  "v_mfma_f32_16x16x32_bf16 v[72:75], a[92:95], v[28:31], v[72:75]\n"
  "v_max_f32 v87, 0, v87\n"
  "v_mul_f32 v86, v86, v90\n"
  "v_mfma_f32_16x16x32_bf16 v[80:83], a[220:223], v[28:31], v[80:83]\n"
  "v_mul_f32 v87, v87, v91\n"
  "v_cvt_pk_bf16_f32 v93, v86, v87\n"
  "v_mfma_f32_16x16x32_bf16 v[72:75], a[96:99], v[32:35], v[72:75]\n"
  "v_mul_f32 v84, v66, v100\n"
  "v_mul_f32 v85, v66, v101\n"
  "v_mfma_f32_16x16x32_bf16 v[80:83], a[224:227], v[32:35], v[80:83]\n"
  "v_mul_f32 v86, v66, v102\n"
  "v_mul_f32 v87, v66, v103\n"
  "v_mfma_f32_16x16x32_bf16 v[72:75], a[100:103], v[36:39], v[72:75]\n"
  "v_mul_f32 v88, v66, v108\n"
  "v_mul_f32 v89, v66, v109\n"
  "v_mfma_f32_16x16x32_bf16 v[80:83], a[228:231], v[36:39], v[80:83]\n"
  "v_mul_f32 v90, v66, v110\n"
  "v_mul_f32 v91, v66, v111\n"
  "v_mfma_f32_16x16x32_bf16 v[72:75], a[104:107], v[40:43], v[72:75]\n"
  "v_mfma_f32_16x16x32_bf16 v[80:83], a[232:235], v[40:43], v[80:83]\n"
  "v_mfma_f32_16x16x32_bf16 v[72:75], a[108:111], v[44:47], v[72:75]\n"
  "v_mfma_f32_16x16x32_bf16 v[80:83], a[236:239], v[44:47], v[80:83]\n"
  "v_mfma_f32_16x16x32_bf16 v[72:75], a[112:115], v[48:51], v[72:75]\n"
  "v_mfma_f32_16x16x32_bf16 v[80:83], a[240:243], v[48:51], v[80:83]\n"
  "v_mfma_f32_16x16x32_bf16 v[72:75], a[116:119], v[52:55], v[72:75]\n"
  "v_mfma_f32_16x16x32_bf16 v[80:83], a[244:247], v[52:55], v[80:83]\n"
  "v_mfma_f32_16x16x32_bf16 v[72:75], a[120:123], v[56:59], v[72:75]\n"
  "v_mfma_f32_16x16x32_bf16 v[80:83], a[248:251], v[56:59], v[80:83]\n"
  "v_mfma_f32_16x16x32_bf16 v[72:75], a[124:127], v[60:63], v[72:75]\n"
  "v_mfma_f32_16x16x32_bf16 v[80:83], a[252:255], v[60:63], v[80:83]\n"
  "s_nop 4\n"
  "v_fmac_f32 v84, v64, v72\n"
  "v_fmac_f32 v85, v64, v73\n"
  "v_fmac_f32 v86, v64, v74\n"
  "v_fmac_f32 v87, v64, v75\n"
  "v_fmac_f32 v88, v64, v80\n"
  "v_fmac_f32 v89, v64, v81\n"
  "v_fmac_f32 v90, v64, v82\n"
  "v_fmac_f32 v91, v64, v83\n"
  "v_cvt_pk_bf16_f32 v84, v84, v85\n"
  "v_cvt_pk_bf16_f32 v86, v86, v87\n"
  "v_cvt_pk_bf16_f32 v88, v88, v89\n"
  "v_cvt_pk_bf16_f32 v90, v90, v91\n"
  "v_and_b32 v85, 0xffff0000, v84\n"
  "v_lshlrev_b32 v84, 16, v84\n"
  "v_and_b32 v89, 0xffff0000, v88\n"
  "v_lshlrev_b32 v88, 16, v88\n"
  "v_max_f32 v84, 0, v84\n"
  "v_max_f32 v85, 0, v85\n"
  "v_mul_f32 v84, v84, v88\n"
  "v_mul_f32 v85, v85, v89\n"
  "v_cvt_pk_bf16_f32 v94, v84, v85\n"
  "v_and_b32 v87, 0xffff0000, v86\n"
  "v_lshlrev_b32 v86, 16, v86\n"
  "v_and_b32 v91, 0xffff0000, v90\n"
  "v_lshlrev_b32 v90, 16, v90\n"
  "v_max_f32 v86, 0, v86\n"
  "v_max_f32 v87, 0, v87\n"
  "v_mul_f32 v86, v86, v90\n"
  "v_mul_f32 v87, v87, v91\n"
  "v_cvt_pk_bf16_f32 v95, v86, v87\n"
  "buffer_store_dwordx4 v[92:95], v120, %[rws], s72 offen sc1\n"
  "v_mov_b32 v0, s66\n"
  "s_mov_b32 s63, s60\n"
  "s_add_u32 s60, s60, 2\n"
  "s_add_u32 s67, s67, 2\n"
  "s_sub_u32 s61, s67, 3\n"
  "s_max_i32 s62, s61, 0\n"
  "s_cmp_ge_u32 s67, 3\n"
  "s_cselect_b32 s67, s62, s67\n"
  "s_lshl_b32 s65, s67, 11\n"
  "s_add_u32 s65, s65, %[pin]\n"
  "s_add_u32 s66, s60, 1\n"
  "s_lshl_b32 s70, s60, 14\n"
  "s_add_u32 s70, s70, m0\n"
  "s_lshl_b32 s71, s60, 11\n"
  "s_add_u32 s71, s71, %[stoff]\n"
  "s_mul_i32 s72, s60, 57344\n"
  "s_add_u32 s72, s72, %[hbase]\n"
  "s_waitcnt vmcnt(0)\n"
  "s_barrier\n"
  "buffer_store_dword v0, off, %[rws], %[pout] sc1\n"
  "s_cmp_lt_u32 s60, 128\n"
  "s_cbranch_scc1 .Lg_top_%=\n"
  "s_endpgm\n"
  ".Lg_spin_%=:\n"
  "s_add_u32 s64, s64, 1\n"
  "s_sleep 1\n"
  "s_cmp_lt_u32 s64, 262144\n"
  "s_cbranch_scc1 .Lg_poll_%=\n"
  "v_mov_b32 v0, 1\n"
  "buffer_store_dword v0, off, %[rws], 0 offset:2048 sc1\n"
  "s_endpgm\n"
      :
      : [rws] "s"(rws), [rx] "s"(rx), "{s60}"(rep), [wave] "s"(wave),
        [pin] "s"(OFF_PW + layer * 24 * PST), [pout] "s"(OFF_PG + ((layer * 2 + rep) * 16 + gi) * PST + (wave ? 128 : 0)),
        [hbase] "s"(OFF_HB + layer * NUNIT * 57344 + (4 * gi + wave) * 1024), [stoff] "s"(OFF_ST1 + layer * 262144),
        [prof] "s"(OFF_PROF + (((role * 4 + wave) * 129) << 7)),
        "{v[96:99]}"(cs[0]), "{v[100:103]}"(cs[1]), "{v[104:107]}"(cs[2]), "{v[108:111]}"(cs[3])
      : "memory", "vcc", "scc", "v0","v1","v2","v3","v4","v5","v6","v7","v8","v9","v10","v11","v12","v13","v14","v15","v16","v17","v18","v19","v20","v21","v22","v23","v24","v25","v26","v27","v28","v29","v30","v31","v32","v33","v34","v35","v36","v37","v38","v39","v40","v41","v42","v43","v44","v45","v46","v47","v48","v49","v50","v51","v52","v53","v54","v55","v56","v57","v58","v59","v60","v61","v62","v63","v64","v65","v66","v67","v68","v69","v70","v71","v72","v73","v74","v75","v76","v77","v78","v79","v80","v81","v82","v83","v84","v85","v86","v87","v88","v89","v90","v91","v92","v93","v94","v95","v112","v113","v114","v115","v116","v117","v118","v119","v120","v121","v122","v123","v124","v125","v126","v127", SCLOB);
  __builtin_unreachable();
}

__global__ void __launch_bounds__(256, 1) __attribute__((amdgpu_num_vgpr(128))) rmt_reglu_kernel(Params p) {
  asm volatile("" ::: "v255", "a255");
  const int tid = __builtin_amdgcn_workitem_id_x(), bid = __builtin_amdgcn_workgroup_id_x();
  const unsigned long long tstart = __builtin_amdgcn_s_memrealtime();
  const int role = (bid & 7) * 32 + (bid >> 3);
  const int wave = rfl(tid >> 6);
  const int layer = role >> 7, xq = (role >> 5) & 3, sl = role & 31;
  const int k = sl - 16;
  if (layer == 1 && sl >= 16) {
    const int sg = xq * 16 + k, lane = tid & 63, row = 8 * wave + (lane >> 3);
    const rsrc_t rx = mkrsrc(p.x, 2048 * 1024), rws = mkrsrc(p.ws, WS_USED);
    v4u xr[8];
#pragma unroll
    for (int i = 0; i < 8; ++i) xr[i] = ld_sc1(rx, row * 1024 + (lane & 7) * 16 + i * 128, sg * 32768);
    float s1 = 0.f, s2 = 0.f;
#pragma unroll
    for (int i = 0; i < 8; ++i)
#pragma unroll
      for (int e = 0; e < 4; ++e) { s1 = dot2(xr[i][e], ONES, s1); s2 = dot2(xr[i][e], xr[i][e], s2); }
    s1 = oct_sum(s1); s2 = oct_sum(s2);
    const bool keep = (lane & 7) == 0;
    const int so = (row >> 4) * 2048 + (lane & 7) * 256 + (row & 15) * 8;
    __builtin_amdgcn_raw_buffer_store_b64(v2u{keep ? f2u(s1) : 0u, keep ? f2u(s2) : 0u}, rws, so, OFF_ST2 + sg * 4096, 16);
    __builtin_amdgcn_raw_buffer_store_b64(v2u{0u, 0u}, rws, so + 128, OFF_ST2 + sg * 4096, 16);
#pragma unroll
    for (int i = 0; i < 8; ++i) st_sc1(v4u{0x98000000u, 0x77FFFFF6u, 0x98000000u, 0x77FFFFF6u}, rws, tid * 16 + i * 4096, OFF_ACC + sg * 32768);
    DRAIN();
    BAR();
    if (wave == 0) count_add(rws, OFF_CNTS, 1u);
  }
  if (sl >= 16) while (__builtin_amdgcn_s_memrealtime() - tstart < HOLD0) __builtin_amdgcn_s_sleep(4);
  if (sl < 16) role_H(p, role, wave, layer, xq >> 1, (xq & 1) * 16 + sl);
  else if (k < 7) role_G(p, role, wave, layer, xq >> 1, (xq & 1) * 7 + k);
  else if (k > 7 && xq < 3) role_W(p, role, wave, layer, xq, k - 8);
  else role_D(p, role, wave, layer, k == 7 ? 2 : (k - 8) >> 2, k == 7 ? xq : k & 3);
}

extern "C" void kernel_launch(void* const* d_in, const int* in_sizes, int n_in, void* d_out, int out_size, void* d_ws, size_t ws_size,
                              hipStream_t stream) {
  (void)in_sizes; (void)n_in; (void)out_size; (void)ws_size;
  Params p;
  p.x = d_in[0]; p.mem0 = d_in[1]; p.wq = d_in[2]; p.wk = d_in[3]; p.wv = d_in[4]; p.wo = d_in[5];
  p.wg = d_in[8]; p.wu = d_in[9]; p.wd = d_in[10];
  p.out = d_out; p.ws = (char*)d_ws;
  (void)hipFuncSetAttribute((const void*)rmt_reglu_kernel, hipFuncAttributeMaxDynamicSharedMemorySize, LDS_BYTES);
  (void)hipMemsetAsync(d_ws, 0, CTL_BYTES, stream);
  hipLaunchKernelGGL(rmt_reglu_kernel, dim3(256), dim3(256), LDS_BYTES, stream, p);
}
```
